```python
import torch

def _softmax(x, axis=-1):
    return torch.softmax(x, dim=axis)
def _gelu(x):
    return torch.nn.functional.gelu(x, approximate="tanh")
def _silu(x):
    return torch.nn.functional.silu(x)
def _sigmoid(x):
    return torch.sigmoid(x)
def _layernorm(x, axis=-1, eps=1e-5):
    m = x.mean(dim=axis, keepdim=True)
    v = x.var(dim=axis, keepdim=True, unbiased=False)
    return (x - m) / torch.sqrt(v + eps)
def _scatter_add(tgt, idx, src, axis=0):
    return tgt.clone().index_add_(axis, idx, src)
def _segment_sum(src, seg, n):
    out = torch.zeros((n,) + src.shape[1:], dtype=src.dtype)
    return out.index_add_(0, seg, src)
def _pad(x, pw):
    flat = tuple(v for pair in reversed(pw) for v in pair)
    return torch.nn.functional.pad(x, flat)

kernel_name = 'sequential_preLN__mha_block_cached__glu_gated_glu__decode'

def setup_inputs():

    _w = (lambda a: (lambda t: t.to(torch.bfloat16) if t.is_floating_point() else t)(torch.as_tensor(a)))
    def _A_setup():
        B, Sq, D = 4, 1, 512
        H, Sk, Dh = 32, 8192, 128
        return {
            "x": _w((lambda *s: torch.randn(*s, dtype=torch.bfloat16) * 0.05)(B, Sq, D)),
            "k_cache": _w((lambda *s: torch.randn(*s, dtype=torch.bfloat16) * 3.2)(B, H, Sk, Dh)),
            "v_cache": _w((lambda *s: torch.randn(*s, dtype=torch.bfloat16) * 0.4)(B, H, Sk, Dh)),
            "wq": _w((lambda *s: torch.randn(*s, dtype=torch.bfloat16) * 3.2)(D, H * Dh) * (D ** -0.5)),
            "wo": _w((lambda *s: torch.randn(*s, dtype=torch.bfloat16) * 0.4)(H * Dh, D) * ((H * Dh) ** -0.5)),
        }
    _A_in = _A_setup()

    def _B_setup():
        B, S, D, F = 4, 1, 512, 1792
        return {
            "x": _w((lambda *s: torch.randn(*s, dtype=torch.bfloat16) * 0.05)(B, S, D)),
            "w_gate": _w((lambda *s: torch.randn(*s, dtype=torch.bfloat16) * 0.1)(D, F)),
            "w_up": _w((lambda *s: torch.randn(*s, dtype=torch.bfloat16) * 0.05)(D, F)),
            "w_down": _w((lambda *s: torch.randn(*s, dtype=torch.bfloat16) * 0.05)(F, D)),
        }
    _B_in = _B_setup()

    return {
        "A_x": _A_in["x"],
        "A_k_cache": _A_in["k_cache"],
        "A_v_cache": _A_in["v_cache"],
        "A_wq": _A_in["wq"],
        "A_wo": _A_in["wo"],
        "B_x": _B_in["x"],
        "B_w_gate": _B_in["w_gate"],
        "B_w_up": _B_in["w_up"],
        "B_w_down": _B_in["w_down"],
    }

def reference(A_x, A_k_cache, A_v_cache, A_wq, A_wo, B_x, B_w_gate, B_w_up, B_w_down):
    def _A_ref(x, k_cache, v_cache, wq, wo):
        B, Sq, D = x.shape
        H, Dh = 32, 128
        q = (lambda x, *d: x.permute(*d))((x @ wq).reshape(B, Sq, H, Dh), 0, 2, 1, 3)
        scores = torch.einsum("bhqd,bhkd->bhqk", q, k_cache) * (Dh ** -0.5)
        p = _softmax(scores, axis=-1)
        o = torch.einsum("bhqk,bhkd->bhqd", p, v_cache)
        return (lambda x, *d: x.permute(*d))(o, 0, 2, 1, 3).reshape(B, Sq, H * Dh) @ wo

    def _B_ref(x, w_gate, w_up, w_down):
        g = torch.einsum("bsd,df->bsf", x, w_gate)
        u = torch.einsum("bsd,df->bsf", x, w_up)
        h = _sigmoid(g) * u
        return torch.einsum("bsf,fd->bsd", h, w_down)

    x = A_x
    h = x + _A_ref(_layernorm(x, axis=-1), A_k_cache, A_v_cache, A_wq, A_wo)
    h2 = h + _B_ref(_layernorm(h, axis=-1), B_w_gate, B_w_up, B_w_down)
    return h2
```

```cpp
#include <hip/hip_runtime.h>
#include <hip/hip_bf16.h>
#include <stdint.h>

#define MIMIC 1

typedef unsigned u32x4 __attribute__((ext_vector_type(4)));
typedef float f32x4 __attribute__((ext_vector_type(4)));
typedef __bf16 bf16x8 __attribute__((ext_vector_type(8)));
typedef __bf16 bf16x2 __attribute__((ext_vector_type(2)));

constexpr int B_ = 4, D_ = 512, H_ = 32, SK = 8192, DH = 128, F_ = 1792;
constexpr int NSPLIT = 2;
constexpr int NWAVE = 8;
constexpr int KEYS_WG = SK / NSPLIT;
constexpr int TILE = 64;
constexpr int NT = KEYS_WG / (TILE * NWAVE);
constexpr float LOG2E = 1.4426950408889634f;

__device__ __forceinline__ float bf2f(unsigned short u) { return __uint_as_float((unsigned)u << 16); }
__device__ __forceinline__ unsigned cvtpk(float lo, float hi) {
  unsigned r; asm("v_cvt_pk_bf16_f32 %0, %1, %2" : "=v"(r) : "v"(lo), "v"(hi)); return r;
}
__device__ __forceinline__ float rbf(float x) { return __uint_as_float(cvtpk(x, x) & 0xffff0000u); }
#define RB(x) rbf(x)
__device__ __forceinline__ float dot2(unsigned k, unsigned q, float acc) {
  return __builtin_amdgcn_fdot2_f32_bf16(__builtin_bit_cast(bf16x2, k), __builtin_bit_cast(bf16x2, q), acc, false);
}
template <int CTRL> __device__ __forceinline__ float dppf(float x) {
  return __builtin_bit_cast(float, __builtin_amdgcn_mov_dpp(__builtin_bit_cast(int, x), CTRL, 0xf, 0xf, true));
}
template <int CTRL> __device__ __forceinline__ unsigned dppu(unsigned x) {
  return (unsigned)__builtin_amdgcn_mov_dpp((int)x, CTRL, 0xf, 0xf, true);
}
constexpr int XOR1 = 0xB1, XOR2 = 0x4E, XOR7 = 0x141, XOR8 = 0x128, BCAST = 0x150;
__device__ __forceinline__ float xrow_max(float x) {
  auto s = __builtin_amdgcn_permlane16_swap(__float_as_uint(x), __float_as_uint(x), false, false);
  x = fmaxf(__uint_as_float(s[0]), __uint_as_float(s[1]));
  auto t = __builtin_amdgcn_permlane32_swap(__float_as_uint(x), __float_as_uint(x), false, false);
  return fmaxf(__uint_as_float(t[0]), __uint_as_float(t[1]));
}
__device__ __forceinline__ float xrow_sum(float x) {
  auto s = __builtin_amdgcn_permlane16_swap(__float_as_uint(x), __float_as_uint(x), false, false);
  x = __uint_as_float(s[0]) + __uint_as_float(s[1]);
  auto t = __builtin_amdgcn_permlane32_swap(__float_as_uint(x), __float_as_uint(x), false, false);
  return __uint_as_float(t[0]) + __uint_as_float(t[1]);
}
__device__ __forceinline__ float wave_max(float x) {
  x = fmaxf(x, dppf<XOR1>(x)); x = fmaxf(x, dppf<XOR2>(x)); x = fmaxf(x, dppf<XOR7>(x)); x = fmaxf(x, dppf<XOR8>(x));
  return xrow_max(x);
}
__device__ __forceinline__ float wave_sum(float x) {
  x += dppf<XOR1>(x); x += dppf<XOR2>(x); x += dppf<XOR7>(x); x += dppf<XOR8>(x);
  return xrow_sum(x);
}
__device__ __forceinline__ f32x4 mfma(u32x4 a, u32x4 b, f32x4 c) {
  return __builtin_amdgcn_mfma_f32_16x16x32_bf16(__builtin_bit_cast(bf16x8, a), __builtin_bit_cast(bf16x8, b), c, 0, 0, 0);
}
__device__ __forceinline__ u32x4 ldnt(const void* p) { return __builtin_nontemporal_load((const u32x4*)p); }
__device__ __forceinline__ u32x4 ld(const void* p) { return *(const u32x4*)p; }

template <int T> __device__ __forceinline__ u32x4 mkA(unsigned pp, const u32x4& mask) {
  unsigned b = dppu<BCAST + T>(pp);
  return u32x4{b & mask[0], b & mask[1], b & mask[2], b & mask[3]};
}

__device__ __forceinline__ void ln_stats(const float (&v)[8], float& mean_r, float& den_r) {
  float s = 0.f;
#pragma unroll
  for (int j = 0; j < 8; ++j) s += v[j];
  s = wave_sum(s);
  const float mean = s * (1.0f / 512.0f);
  float q = 0.f;
#pragma unroll
  for (int j = 0; j < 8; ++j) { const float d = v[j] - mean; q += d * d; }
  q = wave_sum(q);
  const float var = q * (1.0f / 512.0f);
  mean_r = RB(mean);
  den_r = RB(sqrtf(RB(RB(var) + 1e-5f)));
}

constexpr int WS_Y = 0, WS_ML = 256 * 512, WS_H = WS_ML + 512, WS_P = WS_H + 2048;

__global__ void __launch_bounds__(512, 2)
attn_kernel(const unsigned short* __restrict__ x, const unsigned short* __restrict__ kc, const unsigned short* __restrict__ vc,
            const unsigned short* __restrict__ wq, const unsigned short* __restrict__ wo, float* __restrict__ ws) {
  __shared__ __attribute__((aligned(16))) float lds[NWAVE * 132 + 16];
  const int bid = blockIdx.x;
  const int xcd = bid & 7, idx = bid >> 3;
  const int h = xcd * 4 + (idx & 3), b = (idx >> 2) & 3, s = idx >> 4;
  const int tid = threadIdx.x, lane = tid & 63, w = __builtin_amdgcn_readfirstlane(tid >> 6), r = lane >> 4, c = lane & 15;

  const u32x4 xr = ld(x + b * D_ + lane * 8);
  const unsigned short xs = x[b * D_ + 64 * w + 4 * c + r];
  u32x4 Kt[16], Vt[16];
  {
    const unsigned short* wp = wq + (size_t)(64 * w + r) * (H_ * DH) + h * DH + 8 * c;
#pragma unroll
    for (int t = 0; t < 16; ++t) Vt[t] = ld(wp + (size_t)4 * t * (H_ * DH));
  }
  const size_t base = ((size_t)(b * H_ + h) * SK + (size_t)s * KEYS_WG + (size_t)w * TILE) * DH + lane * 8;
  const unsigned short* kp = kc + base;
  const unsigned short* vp = vc + base;
#pragma unroll
  for (int t = 0; t < 16; ++t) Kt[t] = ldnt(kp + t * 512);
  __builtin_amdgcn_sched_barrier(0);

  u32x4 mask;
  {
    const unsigned hm = (c & 1) ? 0xffff0000u : 0x0000ffffu;
#pragma unroll
    for (int i = 0; i < 4; ++i) mask[i] = (c < 8 && (c >> 1) == i) ? hm : 0u;
  }
  u32x4 q;
  {
    float v[8];
#pragma unroll
    for (int j = 0; j < 4; ++j) { v[2 * j] = __uint_as_float(xr[j] << 16); v[2 * j + 1] = __uint_as_float(xr[j] & 0xffff0000u); }
    float mean, den;
    ln_stats(v, mean, den);
    const float xn = RB(bf2f(xs) - mean) / den;
    const unsigned pp = cvtpk(xn, xn);
    f32x4 acc = {0.f, 0.f, 0.f, 0.f};
#define QS(T) acc = mfma(mkA<T>(pp, mask), Vt[T], acc);
    QS(0) QS(1) QS(2) QS(3) QS(4) QS(5) QS(6) QS(7) QS(8) QS(9) QS(10) QS(11) QS(12) QS(13) QS(14) QS(15)
#undef QS
    __builtin_amdgcn_sched_barrier(0);
#pragma unroll
    for (int t = 0; t < 16; ++t) Vt[t] = ldnt(vp + t * 512);
    __builtin_amdgcn_sched_barrier(0);
    if (lane < 32) *(f32x4*)(lds + w * 128 + 8 * c + 4 * r) = acc;
    __syncthreads();
    f32x4 a0 = {0.f, 0.f, 0.f, 0.f}, a1 = {0.f, 0.f, 0.f, 0.f};
#pragma unroll
    for (int ww = 0; ww < NWAVE; ++ww) { a0 += *(const f32x4*)(lds + ww * 128 + 8 * c); a1 += *(const f32x4*)(lds + ww * 128 + 8 * c + 4); }
    q = u32x4{cvtpk(a0[0], a0[1]), cvtpk(a0[2], a0[3]), cvtpk(a1[0], a1[1]), cvtpk(a1[2], a1[3])};
  }

  float m = -INFINITY, l = 0.f;
  f32x4 acc = {0.f, 0.f, 0.f, 0.f};
  const unsigned short* wop = wo + (size_t)(h * DH + 64 * (w >> 2) + r) * D_ + 128 * (w & 3) + 8 * c;
  for (int it = 0; it < NT; ++it) {
    float xx[16];
#pragma unroll
    for (int t = 0; t < 16; ++t) { float d = 0.f;
#pragma unroll
      for (int k = 0; k < 4; ++k) d = dot2(Kt[t][k], q[k], d);
      xx[t] = d; }
    __builtin_amdgcn_sched_barrier(0);
    kp += NWAVE * TILE * DH;
    if (it + 1 < NT) {
#pragma unroll
      for (int t = 0; t < 16; ++t) Kt[t] = ldnt(kp + t * 512);
    } else {
#pragma unroll
      for (int t = 0; t < 16; ++t) Kt[t] = ld(wop + t * 4 * D_);
    }
    __builtin_amdgcn_sched_barrier(0);
#define TR_STEP(HALF, CTRL, BIT) _Pragma("unroll") for (int hh = 0; hh < (HALF); ++hh) { \
      const float lo_ = xx[hh], hi_ = xx[hh + (HALF)]; const float t1_ = lo_ + dppf<CTRL>(lo_), t2_ = hi_ + dppf<CTRL>(hi_); \
      xx[hh] = (c & (BIT)) ? t2_ : t1_; }
    TR_STEP(8, XOR8, 8) TR_STEP(4, XOR7, 4) TR_STEP(2, XOR2, 2) TR_STEP(1, XOR1, 1)
#undef TR_STEP
    const float sc = RB(RB(xx[0]) * 0.08838834764831845f);
    const float mn = fmaxf(m, wave_max(sc));
    const float alpha = __builtin_amdgcn_exp2f((m - mn) * LOG2E);
    const float p = __builtin_amdgcn_exp2f((sc - mn) * LOG2E);
    m = mn;
    l = l * alpha + p;
    acc *= alpha;
    const unsigned pp = cvtpk(p, p);
#define PV(T) acc = mfma(mkA<T>(pp, mask), Vt[T], acc);
    PV(0) PV(1) PV(2) PV(3) PV(4) PV(5) PV(6) PV(7) PV(8) PV(9) PV(10) PV(11) PV(12) PV(13) PV(14) PV(15)
#undef PV
    __builtin_amdgcn_sched_barrier(0);
    vp += NWAVE * TILE * DH;
    if (it + 1 < NT) {
#pragma unroll
      for (int t = 0; t < 16; ++t) Vt[t] = ldnt(vp + t * 512);
    }
    __builtin_amdgcn_sched_barrier(0);
  }
  l = wave_sum(l);
  __syncthreads();
  if (lane < 32) *(f32x4*)(lds + w * 132 + 4 + 8 * c + 4 * r) = acc;
  if (lane == 0) { lds[w * 132] = m; lds[w * 132 + 1] = l; }
  __syncthreads();
  float M = -INFINITY;
#pragma unroll
  for (int ww = 0; ww < NWAVE; ++ww) M = fmaxf(M, lds[ww * 132]);
  float L = 0.f, o = 0.f;
  const int d = 64 * (w >> 2) + 4 * c + r;
#pragma unroll
  for (int ww = 0; ww < NWAVE; ++ww) {
    const float f = __builtin_amdgcn_exp2f((lds[ww * 132] - M) * LOG2E);
    L += f * lds[ww * 132 + 1];
    o += f * lds[ww * 132 + 4 + d];
  }
  {
    const float ohi = rbf(o), olo = o - ohi;
    const unsigned hh = cvtpk(ohi, ohi), ll = cvtpk(olo, olo);
    u32x4 mask2;
    const unsigned hm = (c & 1) ? 0xffff0000u : 0x0000ffffu;
#pragma unroll
    for (int i = 0; i < 4; ++i) mask2[i] = (((c & 7) >> 1) == i) ? hm : 0u;
    f32x4 y = {0.f, 0.f, 0.f, 0.f};
#define WO(T) { const unsigned bh = dppu<BCAST + T>(hh), bl = dppu<BCAST + T>(ll); const unsigned bb = (c < 8) ? bh : bl; \
      y = mfma(u32x4{bb & mask2[0], bb & mask2[1], bb & mask2[2], bb & mask2[3]}, Kt[T], y); }
    WO(0) WO(1) WO(2) WO(3) WO(4) WO(5) WO(6) WO(7) WO(8) WO(9) WO(10) WO(11) WO(12) WO(13) WO(14) WO(15)
#undef WO
#pragma unroll
    for (int i = 0; i < 4; ++i) {
      auto t = __builtin_amdgcn_permlane32_swap(__float_as_uint(y[i]), __float_as_uint(y[i]), false, false);
      y[i] = __uint_as_float(t[0]) + __uint_as_float(t[1]);
    }
    __syncthreads();
    if (lane < 32) *(f32x4*)(lds + w * 128 + 8 * c + 4 * r) = y;
    __syncthreads();
    const int slot = (b * H_ + h) * 2 + s;
    ws[WS_Y + slot * 512 + tid] = lds[tid] + lds[512 + tid];
    if (tid == 0) { ws[WS_ML + slot * 2] = M; ws[WS_ML + slot * 2 + 1] = L; }
  }
}

__global__ void __launch_bounds__(256)
red1_kernel(const unsigned short* __restrict__ x, float* __restrict__ ws) {
  __shared__ float sw[64];
  __shared__ float part[4][64];
  const int b = blockIdx.x >> 3, cg = blockIdx.x & 7, tid = threadIdx.x;
  if (tid < 64) {
    const int hh = tid >> 1;
    const float m0 = ws[WS_ML + (b * 64 + hh * 2) * 2], l0 = ws[WS_ML + (b * 64 + hh * 2) * 2 + 1];
    const float m1 = ws[WS_ML + (b * 64 + hh * 2 + 1) * 2], l1 = ws[WS_ML + (b * 64 + hh * 2 + 1) * 2 + 1];
    const float M = fmaxf(m0, m1);
    const float f0 = __builtin_amdgcn_exp2f((m0 - M) * LOG2E), f1 = __builtin_amdgcn_exp2f((m1 - M) * LOG2E);
    const float L = f0 * l0 + f1 * l1;
    sw[tid] = ((tid & 1) ? f1 : f0) / L;
  }
  __syncthreads();
  const int col = cg * 64 + (tid & 63), g = tid >> 6;
  float a = 0.f;
#pragma unroll
  for (int p = 0; p < 16; ++p) a += sw[g * 16 + p] * ws[WS_Y + (b * 64 + g * 16 + p) * 512 + col];
  part[g][tid & 63] = a;
  __syncthreads();
  if (tid < 64) {
    const float at = part[0][tid] + part[1][tid] + part[2][tid] + part[3][tid];
    ws[WS_H + b * 512 + col] = RB(bf2f(x[b * 512 + col]) + RB(at));
  }
}

__global__ void __launch_bounds__(512)
mlp_kernel(const unsigned short* __restrict__ wg, const unsigned short* __restrict__ wu, const unsigned short* __restrict__ wd,
           float* __restrict__ ws) {
  __shared__ float hn[512];
  __shared__ float part[16][64];
  __shared__ float hid[32];
  const int b = blockIdx.x / 56, sl = blockIdx.x % 56, tid = threadIdx.x, lane = tid & 63;
  float v[8];
#pragma unroll
  for (int j = 0; j < 8; ++j) v[j] = ws[WS_H + b * 512 + lane * 8 + j];
  float mean, den;
  ln_stats(v, mean, den);
  hn[tid] = RB(RB(ws[WS_H + b * 512 + tid] - mean) / den);
  __syncthreads();
  {
    const int col = tid & 31, mat = (tid >> 5) & 1, kg = tid >> 6;
    const unsigned short* wp = (mat ? wu : wg) + (size_t)(kg * 64) * F_ + sl * 32 + col;
    float a = 0.f;
#pragma unroll 16
    for (int k = 0; k < 64; ++k) a += hn[kg * 64 + k] * bf2f(wp[(size_t)k * F_]);
    part[kg][tid & 63] = a;
  }
  __syncthreads();
  if (tid < 32) {
    float g = 0.f, u = 0.f;
#pragma unroll
    for (int k = 0; k < 8; ++k) { g += part[k][tid]; u += part[k][32 + tid]; }
    g = RB(g); u = RB(u);
    const float sg = RB(1.0f / (1.0f + __expf(-g)));
    hid[tid] = RB(sg * u);
  }
  __syncthreads();
  float a = 0.f;
#pragma unroll
  for (int f = 0; f < 32; ++f) a += hid[f] * bf2f(wd[(size_t)(sl * 32 + f) * 512 + tid]);
  ws[WS_P + (b * 56 + sl) * 512 + tid] = a;
}

__global__ void __launch_bounds__(256)
red2_kernel(const float* __restrict__ ws, unsigned short* __restrict__ out) {
  __shared__ float part[4][64];
  const int b = blockIdx.x >> 3, cg = blockIdx.x & 7, tid = threadIdx.x;
  const int col = cg * 64 + (tid & 63), g = tid >> 6;
  float a = 0.f;
#pragma unroll
  for (int p = 0; p < 14; ++p) a += ws[WS_P + (b * 56 + g * 14 + p) * 512 + col];
  part[g][tid & 63] = a;
  __syncthreads();
  if (tid < 64) {
    const float at = part[0][tid] + part[1][tid] + part[2][tid] + part[3][tid];
    const float o = ws[WS_H + b * 512 + col] + RB(at);
    out[b * 512 + col] = (unsigned short)(cvtpk(o, o) & 0xffffu);
  }
}

extern "C" void kernel_launch(void* const* d_in, const int* in_sizes, int n_in, void* d_out, int out_size, void* d_ws,
                              size_t ws_size, hipStream_t stream) {
  const unsigned short* x = (const unsigned short*)d_in[0];
  const unsigned short* kc = (const unsigned short*)d_in[1];
  const unsigned short* vc = (const unsigned short*)d_in[2];
  const unsigned short* wq = (const unsigned short*)d_in[3];
  const unsigned short* wo = (const unsigned short*)d_in[4];
  const unsigned short* wg = (const unsigned short*)d_in[6];
  const unsigned short* wu = (const unsigned short*)d_in[7];
  const unsigned short* wd = (const unsigned short*)d_in[8];
  float* ws = (float*)d_ws;
  attn_kernel<<<256, 512, 0, stream>>>(x, kc, vc, wq, wo, ws);
  red1_kernel<<<32, 256, 0, stream>>>(x, ws);
  mlp_kernel<<<224, 512, 0, stream>>>(wg, wu, wd, ws);
  red2_kernel<<<32, 256, 0, stream>>>(ws, (unsigned short*)d_out);
}
```

```cpp
#include <hip/hip_runtime.h>
#include <hip/hip_bf16.h>
#include <stdint.h>

#define KAUX 0
#define VAUX 2
#define MIMIC 1

typedef unsigned u32x4 __attribute__((ext_vector_type(4)));
typedef float f32x4 __attribute__((ext_vector_type(4)));
typedef __bf16 bf16x8 __attribute__((ext_vector_type(8)));
typedef __bf16 bf16x2 __attribute__((ext_vector_type(2)));

constexpr int B_ = 4, D_ = 512, H_ = 32, SK = 8192, DH = 128, F_ = 1792;
constexpr int NSPLIT = 2;
constexpr int NWAVE = 8;
constexpr int KEYS_WG = SK / NSPLIT;
constexpr int TILE = 64;
constexpr int NT = KEYS_WG / (TILE * NWAVE);
constexpr float LOG2E = 1.4426950408889634f;

__device__ __forceinline__ float bf2f(unsigned short u) { return __uint_as_float((unsigned)u << 16); }
__device__ __forceinline__ unsigned cvtpk(float lo, float hi) {
  unsigned r; asm("v_cvt_pk_bf16_f32 %0, %1, %2" : "=v"(r) : "v"(lo), "v"(hi)); return r;
}
__device__ __forceinline__ float rbf(float x) { return __uint_as_float(cvtpk(x, x) & 0xffff0000u); }
#define RB(x) rbf(x)
__device__ __forceinline__ float dot2(unsigned k, unsigned q, float acc) {
  return __builtin_amdgcn_fdot2_f32_bf16(__builtin_bit_cast(bf16x2, k), __builtin_bit_cast(bf16x2, q), acc, false);
}
template <int CTRL> __device__ __forceinline__ float dppf(float x) {
  return __builtin_bit_cast(float, __builtin_amdgcn_mov_dpp(__builtin_bit_cast(int, x), CTRL, 0xf, 0xf, true));
}
template <int CTRL> __device__ __forceinline__ unsigned dppu(unsigned x) {
  return (unsigned)__builtin_amdgcn_mov_dpp((int)x, CTRL, 0xf, 0xf, true);
}
constexpr int XOR1 = 0xB1, XOR2 = 0x4E, XOR7 = 0x141, XOR8 = 0x128, BCAST = 0x150;
__device__ __forceinline__ float xrow_max(float x) {
  auto s = __builtin_amdgcn_permlane16_swap(__float_as_uint(x), __float_as_uint(x), false, false);
  x = fmaxf(__uint_as_float(s[0]), __uint_as_float(s[1]));
  auto t = __builtin_amdgcn_permlane32_swap(__float_as_uint(x), __float_as_uint(x), false, false);
  return fmaxf(__uint_as_float(t[0]), __uint_as_float(t[1]));
}
__device__ __forceinline__ float xrow_sum(float x) {
  auto s = __builtin_amdgcn_permlane16_swap(__float_as_uint(x), __float_as_uint(x), false, false);
  x = __uint_as_float(s[0]) + __uint_as_float(s[1]);
  auto t = __builtin_amdgcn_permlane32_swap(__float_as_uint(x), __float_as_uint(x), false, false);
  return __uint_as_float(t[0]) + __uint_as_float(t[1]);
}
__device__ __forceinline__ float wave_max(float x) {
  x = fmaxf(x, dppf<XOR1>(x)); x = fmaxf(x, dppf<XOR2>(x)); x = fmaxf(x, dppf<XOR7>(x)); x = fmaxf(x, dppf<XOR8>(x));
  return xrow_max(x);
}
__device__ __forceinline__ float wave_sum(float x) {
  x += dppf<XOR1>(x); x += dppf<XOR2>(x); x += dppf<XOR7>(x); x += dppf<XOR8>(x);
  return xrow_sum(x);
}
__device__ __forceinline__ f32x4 mfma(u32x4 a, u32x4 b, f32x4 c) {
  return __builtin_amdgcn_mfma_f32_16x16x32_bf16(__builtin_bit_cast(bf16x8, a), __builtin_bit_cast(bf16x8, b), c, 0, 0, 0);
}
__device__ __forceinline__ u32x4 ldnt(const void* p) { return __builtin_nontemporal_load((const u32x4*)p); }
__device__ __forceinline__ u32x4 ld(const void* p) { return *(const u32x4*)p; }

template <int T> __device__ __forceinline__ u32x4 mkA(unsigned pp, const u32x4& mask) {
  unsigned b = dppu<BCAST + T>(pp);
  return u32x4{b & mask[0], b & mask[1], b & mask[2], b & mask[3]};
}

__device__ __forceinline__ void ln_stats(const float (&v)[8], float& mean_r, float& den_r) {
  float s = 0.f;
#pragma unroll
  for (int j = 0; j < 8; ++j) s += v[j];
  s = wave_sum(s);
  const float mean = s * (1.0f / 512.0f);
  float q = 0.f;
#pragma unroll
  for (int j = 0; j < 8; ++j) { const float d = v[j] - mean; q += d * d; }
  q = wave_sum(q);
  const float var = q * (1.0f / 512.0f);
  mean_r = RB(mean);
  den_r = RB(sqrtf(RB(RB(var) + 1e-5f)));
}

constexpr int WS_Y = 0, WS_ML = 256 * 512, WS_H = WS_ML + 512, WS_P = WS_H + 2048;

__global__ void __launch_bounds__(512, 2)
attn_kernel(const unsigned short* __restrict__ x, const unsigned short* __restrict__ kc, const unsigned short* __restrict__ vc,
            const unsigned short* __restrict__ wq, const unsigned short* __restrict__ wo, float* __restrict__ ws) {
  __shared__ __attribute__((aligned(16))) float lds[NWAVE * 132 + 16];
  const int bid = blockIdx.x;
  const int xcd = bid & 7, idx = bid >> 3;
  const int h = xcd * 4 + (idx & 3), b = (idx >> 2) & 3, s = idx >> 4;
  const int tid = threadIdx.x, lane = tid & 63, w = __builtin_amdgcn_readfirstlane(tid >> 6), r = lane >> 4, c = lane & 15;

  const u32x4 xr = ld(x + b * D_ + lane * 8);
  const unsigned short xs = x[b * D_ + 64 * w + 4 * c + r];
  u32x4 Kt[16], Vt[16];
  {
    const unsigned short* wp = wq + (size_t)(64 * w + r) * (H_ * DH) + h * DH + 8 * c;
#pragma unroll
    for (int t = 0; t < 16; ++t) Vt[t] = ld(wp + (size_t)4 * t * (H_ * DH));
  }
  const size_t base = ((size_t)(b * H_ + h) * SK + (size_t)s * KEYS_WG) * DH;
  const __amdgpu_buffer_rsrc_t rk = __builtin_amdgcn_make_buffer_rsrc((void*)(kc + base), 0, KEYS_WG * DH * 2, 0x00020000);
  const __amdgpu_buffer_rsrc_t rv = __builtin_amdgcn_make_buffer_rsrc((void*)(vc + base), 0, KEYS_WG * DH * 2, 0x00020000);
  const int voff = lane * 16;
  int so = w * 8192;
#define TOFF(t) ((((t) >> 3) * 65536) + (((t) & 7) * 1024))
#define LDK(t) Kt[t] = __builtin_amdgcn_raw_buffer_load_b128(rk, voff + ((t) & 3) * 1024, so + TOFF(t) - ((t) & 3) * 1024, KAUX)
#define LDV(t) Vt[t] = __builtin_amdgcn_raw_buffer_load_b128(rv, voff + ((t) & 3) * 1024, so + TOFF(t) - ((t) & 3) * 1024, VAUX)
#pragma unroll
  for (int t = 0; t < 16; ++t) LDK(t);
  __builtin_amdgcn_sched_barrier(0);

  u32x4 mask;
  {
    const unsigned hm = (c & 1) ? 0xffff0000u : 0x0000ffffu;
#pragma unroll
    for (int i = 0; i < 4; ++i) mask[i] = (c < 8 && (c >> 1) == i) ? hm : 0u;
  }
  u32x4 q;
  {
    float v[8];
#pragma unroll
    for (int j = 0; j < 4; ++j) { v[2 * j] = __uint_as_float(xr[j] << 16); v[2 * j + 1] = __uint_as_float(xr[j] & 0xffff0000u); }
    float mean, den;
    ln_stats(v, mean, den);
    const float xn = RB(bf2f(xs) - mean) / den;
    const unsigned pp = cvtpk(xn, xn);
    f32x4 acc = {0.f, 0.f, 0.f, 0.f};
#define QS(T) acc = mfma(mkA<T>(pp, mask), Vt[T], acc);
    QS(0) QS(1) QS(2) QS(3) QS(4) QS(5) QS(6) QS(7) QS(8) QS(9) QS(10) QS(11) QS(12) QS(13) QS(14) QS(15)
#undef QS
    __builtin_amdgcn_sched_barrier(0);
#pragma unroll
    for (int t = 0; t < 16; ++t) LDV(t);
    __builtin_amdgcn_sched_barrier(0);
    if (lane < 32) *(f32x4*)(lds + w * 128 + 8 * c + 4 * r) = acc;
    __syncthreads();
    f32x4 a0 = {0.f, 0.f, 0.f, 0.f}, a1 = {0.f, 0.f, 0.f, 0.f};
#pragma unroll
    for (int ww = 0; ww < NWAVE; ++ww) { a0 += *(const f32x4*)(lds + ww * 128 + 8 * c); a1 += *(const f32x4*)(lds + ww * 128 + 8 * c + 4); }
    q = u32x4{cvtpk(a0[0], a0[1]), cvtpk(a0[2], a0[3]), cvtpk(a1[0], a1[1]), cvtpk(a1[2], a1[3])};
  }

  float m = -INFINITY, l = 0.f;
  f32x4 acc = {0.f, 0.f, 0.f, 0.f};
  const unsigned short* wop = wo + (size_t)(h * DH + 64 * (w >> 2) + r) * D_ + 128 * (w & 3) + 8 * c;
#define TR_STEP(HALF, CTRL, BIT) _Pragma("unroll") for (int hh = 0; hh < (HALF); ++hh) { \
      const float lo_ = xx[hh], hi_ = xx[hh + (HALF)]; const float t1_ = lo_ + dppf<CTRL>(lo_), t2_ = hi_ + dppf<CTRL>(hi_); \
      xx[hh] = (c & (BIT)) ? t2_ : t1_; }
#define PV(T) acc = mfma(mkA<T>(pp, mask), Vt[T], acc);
#define BODY(LAST) { \
    float xx[16]; \
    so += 131072; \
    _Pragma("unroll") for (int t = 0; t < 16; ++t) { float d = 0.f; \
      _Pragma("unroll") for (int k = 0; k < 4; ++k) d = dot2(Kt[t][k], q[k], d); \
      xx[t] = d; \
      __builtin_amdgcn_sched_barrier(0); \
      if (!(LAST)) LDK(t); else Kt[t] = ld(wop + t * 4 * D_); \
      __builtin_amdgcn_sched_barrier(0); } \
    TR_STEP(8, XOR8, 8) TR_STEP(4, XOR7, 4) TR_STEP(2, XOR2, 2) TR_STEP(1, XOR1, 1) \
    const float sc = RB(RB(xx[0]) * 0.08838834764831845f); \
    const float mn = fmaxf(m, wave_max(sc)); \
    const float alpha = __builtin_amdgcn_exp2f((m - mn) * LOG2E); \
    const float p = __builtin_amdgcn_exp2f((sc - mn) * LOG2E); \
    m = mn; \
    l = l * alpha + p; \
    acc *= alpha; \
    const unsigned pp = cvtpk(p, p); \
    __builtin_amdgcn_sched_barrier(0); \
    PV(0) PV(1) PV(2) PV(3) __builtin_amdgcn_sched_barrier(0); if (!(LAST)) { LDV(0); LDV(1); LDV(2); LDV(3); } __builtin_amdgcn_sched_barrier(0); \
    PV(4) PV(5) PV(6) PV(7) __builtin_amdgcn_sched_barrier(0); if (!(LAST)) { LDV(4); LDV(5); LDV(6); LDV(7); } __builtin_amdgcn_sched_barrier(0); \
    PV(8) PV(9) PV(10) PV(11) __builtin_amdgcn_sched_barrier(0); if (!(LAST)) { LDV(8); LDV(9); LDV(10); LDV(11); } __builtin_amdgcn_sched_barrier(0); \
    PV(12) PV(13) PV(14) PV(15) __builtin_amdgcn_sched_barrier(0); if (!(LAST)) { LDV(12); LDV(13); LDV(14); LDV(15); } __builtin_amdgcn_sched_barrier(0); \
  }
  for (int it = 0; it < NT - 1; ++it) BODY(false)
  BODY(true)
  l = wave_sum(l);
  __syncthreads();
  if (lane < 32) *(f32x4*)(lds + w * 132 + 4 + 8 * c + 4 * r) = acc;
  if (lane == 0) { lds[w * 132] = m; lds[w * 132 + 1] = l; }
  __syncthreads();
  float M = -INFINITY;
#pragma unroll
  for (int ww = 0; ww < NWAVE; ++ww) M = fmaxf(M, lds[ww * 132]);
  float L = 0.f, o = 0.f;
  const int d = 64 * (w >> 2) + 4 * c + r;
#pragma unroll
  for (int ww = 0; ww < NWAVE; ++ww) {
    const float f = __builtin_amdgcn_exp2f((lds[ww * 132] - M) * LOG2E);
    L += f * lds[ww * 132 + 1];
    o += f * lds[ww * 132 + 4 + d];
  }
  {
    const float ohi = rbf(o), olo = o - ohi;
    const unsigned hh = cvtpk(ohi, ohi), ll = cvtpk(olo, olo);
    u32x4 mask2;
    const unsigned hm = (c & 1) ? 0xffff0000u : 0x0000ffffu;
#pragma unroll
    for (int i = 0; i < 4; ++i) mask2[i] = (((c & 7) >> 1) == i) ? hm : 0u;
    f32x4 y = {0.f, 0.f, 0.f, 0.f};
#define WO(T) { const unsigned bh = dppu<BCAST + T>(hh), bl = dppu<BCAST + T>(ll); const unsigned bb = (c < 8) ? bh : bl; \
      y = mfma(u32x4{bb & mask2[0], bb & mask2[1], bb & mask2[2], bb & mask2[3]}, Kt[T], y); }
    WO(0) WO(1) WO(2) WO(3) WO(4) WO(5) WO(6) WO(7) WO(8) WO(9) WO(10) WO(11) WO(12) WO(13) WO(14) WO(15)
#undef WO
#pragma unroll
    for (int i = 0; i < 4; ++i) {
      auto t = __builtin_amdgcn_permlane32_swap(__float_as_uint(y[i]), __float_as_uint(y[i]), false, false);
      y[i] = __uint_as_float(t[0]) + __uint_as_float(t[1]);
    }
    __syncthreads();
    if (lane < 32) *(f32x4*)(lds + w * 128 + 8 * c + 4 * r) = y;
    __syncthreads();
    const int slot = (b * H_ + h) * 2 + s;
    ws[WS_Y + slot * 512 + tid] = lds[tid] + lds[512 + tid];
    if (tid == 0) { ws[WS_ML + slot * 2] = M; ws[WS_ML + slot * 2 + 1] = L; }
  }
}

__global__ void __launch_bounds__(256)
red1_kernel(const unsigned short* __restrict__ x, float* __restrict__ ws) {
  __shared__ float sw[64];
  __shared__ float part[4][64];
  const int b = blockIdx.x >> 3, cg = blockIdx.x & 7, tid = threadIdx.x;
  if (tid < 64) {
    const int hh = tid >> 1;
    const float m0 = ws[WS_ML + (b * 64 + hh * 2) * 2], l0 = ws[WS_ML + (b * 64 + hh * 2) * 2 + 1];
    const float m1 = ws[WS_ML + (b * 64 + hh * 2 + 1) * 2], l1 = ws[WS_ML + (b * 64 + hh * 2 + 1) * 2 + 1];
    const float M = fmaxf(m0, m1);
    const float f0 = __builtin_amdgcn_exp2f((m0 - M) * LOG2E), f1 = __builtin_amdgcn_exp2f((m1 - M) * LOG2E);
    const float L = f0 * l0 + f1 * l1;
    sw[tid] = ((tid & 1) ? f1 : f0) / L;
  }
  __syncthreads();
  const int col = cg * 64 + (tid & 63), g = tid >> 6;
  float a = 0.f;
#pragma unroll
  for (int p = 0; p < 16; ++p) a += sw[g * 16 + p] * ws[WS_Y + (b * 64 + g * 16 + p) * 512 + col];
  part[g][tid & 63] = a;
  __syncthreads();
  if (tid < 64) {
    const float at = part[0][tid] + part[1][tid] + part[2][tid] + part[3][tid];
    ws[WS_H + b * 512 + col] = RB(bf2f(x[b * 512 + col]) + RB(at));
  }
}

__global__ void __launch_bounds__(512)
mlp_kernel(const unsigned short* __restrict__ wg, const unsigned short* __restrict__ wu, const unsigned short* __restrict__ wd,
           float* __restrict__ ws) {
  __shared__ float hn[512];
  __shared__ float part[16][64];
  __shared__ float hid[32];
  const int b = blockIdx.x / 56, sl = blockIdx.x % 56, tid = threadIdx.x, lane = tid & 63;
  float v[8];
#pragma unroll
  for (int j = 0; j < 8; ++j) v[j] = ws[WS_H + b * 512 + lane * 8 + j];
  float mean, den;
  ln_stats(v, mean, den);
  hn[tid] = RB(RB(ws[WS_H + b * 512 + tid] - mean) / den);
  __syncthreads();
  {
    const int col = tid & 31, mat = (tid >> 5) & 1, kg = tid >> 6;
    const unsigned short* wp = (mat ? wu : wg) + (size_t)(kg * 64) * F_ + sl * 32 + col;
    float a = 0.f;
#pragma unroll 16
    for (int k = 0; k < 64; ++k) a += hn[kg * 64 + k] * bf2f(wp[(size_t)k * F_]);
    part[kg][tid & 63] = a;
  }
  __syncthreads();
  if (tid < 32) {
    float g = 0.f, u = 0.f;
#pragma unroll
    for (int k = 0; k < 8; ++k) { g += part[k][tid]; u += part[k][32 + tid]; }
    g = RB(g); u = RB(u);
    const float sg = RB(1.0f / (1.0f + __expf(-g)));
    hid[tid] = RB(sg * u);
  }
  __syncthreads();
  float a = 0.f;
#pragma unroll
  for (int f = 0; f < 32; ++f) a += hid[f] * bf2f(wd[(size_t)(sl * 32 + f) * 512 + tid]);
  ws[WS_P + (b * 56 + sl) * 512 + tid] = a;
}

__global__ void __launch_bounds__(256)
red2_kernel(const float* __restrict__ ws, unsigned short* __restrict__ out) {
  __shared__ float part[4][64];
  const int b = blockIdx.x >> 3, cg = blockIdx.x & 7, tid = threadIdx.x;
  const int col = cg * 64 + (tid & 63), g = tid >> 6;
  float a = 0.f;
#pragma unroll
  for (int p = 0; p < 14; ++p) a += ws[WS_P + (b * 56 + g * 14 + p) * 512 + col];
  part[g][tid & 63] = a;
  __syncthreads();
  if (tid < 64) {
    const float at = part[0][tid] + part[1][tid] + part[2][tid] + part[3][tid];
    const float o = ws[WS_H + b * 512 + col] + RB(at);
    out[b * 512 + col] = (unsigned short)(cvtpk(o, o) & 0xffffu);
  }
}

extern "C" void kernel_launch(void* const* d_in, const int* in_sizes, int n_in, void* d_out, int out_size, void* d_ws,
                              size_t ws_size, hipStream_t stream) {
  const unsigned short* x = (const unsigned short*)d_in[0];
  const unsigned short* kc = (const unsigned short*)d_in[1];
  const unsigned short* vc = (const unsigned short*)d_in[2];
  const unsigned short* wq = (const unsigned short*)d_in[3];
  const unsigned short* wo = (const unsigned short*)d_in[4];
  const unsigned short* wg = (const unsigned short*)d_in[6];
  const unsigned short* wu = (const unsigned short*)d_in[7];
  const unsigned short* wd = (const unsigned short*)d_in[8];
  float* ws = (float*)d_ws;
  attn_kernel<<<256, 512, 0, stream>>>(x, kc, vc, wq, wo, ws);
  red1_kernel<<<32, 256, 0, stream>>>(x, ws);
  mlp_kernel<<<224, 512, 0, stream>>>(wg, wu, wd, ws);
  red2_kernel<<<32, 256, 0, stream>>>(ws, (unsigned short*)d_out);
}
```

```cpp
#include <hip/hip_runtime.h>
#include <hip/hip_bf16.h>
#include <stdint.h>

#define CFG_T 8
#define CFG_W 8
#define CFG_GK 4
#define CFG_BM 3
#define CFG_STAMP 0

typedef unsigned u32x4 __attribute__((ext_vector_type(4)));
typedef unsigned u32x2 __attribute__((ext_vector_type(2)));
typedef float f32x4 __attribute__((ext_vector_type(4)));
typedef float f32x2 __attribute__((ext_vector_type(2)));
typedef __bf16 bf16x8 __attribute__((ext_vector_type(8)));
typedef __bf16 bf16x2 __attribute__((ext_vector_type(2)));
typedef unsigned short u16;

constexpr int D_ = 512, H_ = 32, SK = 8192, DH = 128, F_ = 1792;
constexpr int NW = 8, T = CFG_T, W = CFG_W, GK = CFG_GK;
constexpr int CHUNK_B = (SK / 2) * DH * 2;
constexpr int STEP_B = T * NW * 1024;
constexpr int NTILE = CHUNK_B / STEP_B;
constexpr int NSL = F_ / 32;
constexpr int REP = 8;
constexpr float LOG2E = 1.4426950408889634f;
static_assert((2 * T) % W == 0 && (T == 8 || T == 16) && T % GK == 0, "");

constexpr int WS_EPOCH = 0;
constexpr int WS_HB = 4096;
constexpr int WS_YB = WS_HB + 4 * REP * 2048;
constexpr int WS_PB = WS_YB + 4 * 64 * 5 * 1024;
constexpr int WS_END = WS_PB + 4 * 64 * 4 * 1024;
constexpr int WS_STAMP = 4 << 20;
constexpr int AUX_NT = 2, AUX_SC1 = 16;

template <int N> struct IC { static constexpr int v = N; };
template <int I, int N, typename F> __device__ __forceinline__ void static_for(F&& f) {
  if constexpr (I < N) { f(IC<I>{}); static_for<I + 1, N>(f); }
}

__device__ __forceinline__ float bf2f(u16 u) { return __uint_as_float((unsigned)u << 16); }
__device__ __forceinline__ float bflo(unsigned u) { return __uint_as_float(u << 16); }
__device__ __forceinline__ float bfhi(unsigned u) { return __uint_as_float(u & 0xffff0000u); }
__device__ __forceinline__ unsigned cvtpk(float lo, float hi) {
  return __builtin_bit_cast(unsigned, __builtin_convertvector(f32x2{lo, hi}, bf16x2));
}
__device__ __forceinline__ float rb(float x) { return (float)(__bf16)x; }
__device__ __forceinline__ float dot2(unsigned k, unsigned q, float acc) {
  return __builtin_amdgcn_fdot2_f32_bf16(__builtin_bit_cast(bf16x2, k), __builtin_bit_cast(bf16x2, q), acc, false);
}
template <int CTRL> __device__ __forceinline__ float dppf(float x) {
  return __builtin_bit_cast(float, __builtin_amdgcn_mov_dpp(__builtin_bit_cast(int, x), CTRL, 0xf, 0xf, true));
}
template <int CTRL> __device__ __forceinline__ unsigned dppu(unsigned x) {
  return (unsigned)__builtin_amdgcn_mov_dpp((int)x, CTRL, 0xf, 0xf, true);
}
constexpr int XOR1 = 0xB1, XOR2 = 0x4E, XOR7 = 0x141, XOR8 = 0x128, BCAST = 0x150;
template <int A> __device__ __forceinline__ unsigned bcast2(unsigned x) {
  const int lo = __builtin_amdgcn_mov_dpp((int)x, BCAST + A, 0xf, 0xf, true);
  return (unsigned)__builtin_amdgcn_update_dpp(lo, (int)x, BCAST + A + 1, 0xf, 0xc, false);
}
__device__ __forceinline__ float xrow_max(float x) {
  auto s = __builtin_amdgcn_permlane16_swap(__float_as_uint(x), __float_as_uint(x), false, false);
  x = fmaxf(__uint_as_float(s[0]), __uint_as_float(s[1]));
  auto t = __builtin_amdgcn_permlane32_swap(__float_as_uint(x), __float_as_uint(x), false, false);
  return fmaxf(__uint_as_float(t[0]), __uint_as_float(t[1]));
}
__device__ __forceinline__ float x32_sum(float x) {
  auto t = __builtin_amdgcn_permlane32_swap(__float_as_uint(x), __float_as_uint(x), false, false);
  return __uint_as_float(t[0]) + __uint_as_float(t[1]);
}
__device__ __forceinline__ float xrow_sum(float x) {
  auto s = __builtin_amdgcn_permlane16_swap(__float_as_uint(x), __float_as_uint(x), false, false);
  return x32_sum(__uint_as_float(s[0]) + __uint_as_float(s[1]));
}
__device__ __forceinline__ float wave_sum(float x) {
  x += dppf<XOR1>(x); x += dppf<XOR2>(x); x += dppf<XOR7>(x); x += dppf<XOR8>(x);
  return xrow_sum(x);
}
__device__ __forceinline__ f32x4 mfma(u32x4 a, u32x4 b, f32x4 c) {
  return __builtin_amdgcn_mfma_f32_16x16x32_bf16(__builtin_bit_cast(bf16x8, a), __builtin_bit_cast(bf16x8, b), c, 0, 0, 0);
}
__device__ __forceinline__ u32x4 ld16(const void* p) { return *(const u32x4*)p; }
__device__ __forceinline__ u32x4 band(unsigned b, const u32x4& m) { return u32x4{b & m[0], b & m[1], b & m[2], b & m[3]}; }
#define SB() __builtin_amdgcn_sched_barrier(0)

__device__ __forceinline__ void ln_stats(const float (&v)[8], float& mean_r, float& den_r) {
  float s = 0.f;
#pragma unroll
  for (int j = 0; j < 8; ++j) s += v[j];
  const float mean = wave_sum(s) * (1.0f / 512.0f);
  float q = 0.f;
#pragma unroll
  for (int j = 0; j < 8; ++j) { const float d = v[j] - mean; q += d * d; }
  const float var = wave_sum(q) * (1.0f / 512.0f);
  mean_r = rb(mean);
  den_r = rb(sqrtf(rb(rb(var) + 1e-5f)));
}
__device__ __forceinline__ f32x4 fold_bd(f32x4 d, int lane) {
  f32x4 r;
#pragma unroll
  for (int i = 0; i < 4; ++i) { const float t = dppf<XOR8>(d[i]); r[i] = x32_sum(lane < 32 ? d[i] : t); }
  return r;
}
__device__ __forceinline__ float reduce8(float (&z)[8], int c) {
#pragma unroll
  for (int i = 0; i < 4; ++i) { const float a = z[i] + dppf<XOR8>(z[i]), b = z[i + 4] + dppf<XOR8>(z[i + 4]); z[i] = (c & 8) ? b : a; }
#pragma unroll
  for (int i = 0; i < 2; ++i) { const float a = z[i] + dppf<XOR7>(z[i]), b = z[i + 2] + dppf<XOR7>(z[i + 2]); z[i] = (c & 4) ? b : a; }
  { const float a = z[0] + dppf<XOR2>(z[0]), b = z[1] + dppf<XOR2>(z[1]); z[0] = (c & 2) ? b : a; }
  return xrow_sum(z[0] + dppf<XOR1>(z[0]));
}
template <int N> __device__ __forceinline__ void sweep(__amdgpu_buffer_rsrc_t rs, int voff, int soff, unsigned tag, bool active, u32x4 (&v)[N]) {
  const unsigned long long t0 = __builtin_amdgcn_s_memrealtime();
  for (;;) {
    bool ok = true;
    asm volatile("" ::: "memory");
#pragma unroll
    for (int i = 0; i < N; ++i) v[i] = __builtin_amdgcn_raw_buffer_load_b128(rs, voff + i * 1024, soff, AUX_SC1);
#pragma unroll
    for (int i = 0; i < N; ++i) ok &= (v[i][1] == tag) & (v[i][3] == tag);
    if (__builtin_amdgcn_ballot_w64(active && !ok) == 0ull) break;
    if (__builtin_amdgcn_s_memrealtime() - t0 > 5000000ull) break;
    __builtin_amdgcn_s_sleep(2);
  }
}

#define STAMP(i) do {} while (0)

__global__ void __launch_bounds__(512, 2)
fused_kernel(const u16* __restrict__ x, const u16* __restrict__ kc, const u16* __restrict__ vc, const u16* __restrict__ wq,
             const u16* __restrict__ wo, const u16* __restrict__ wg, const u16* __restrict__ wu, const u16* __restrict__ wd,
             u16* __restrict__ out, unsigned char* __restrict__ ws) {
  __shared__ __attribute__((aligned(16))) float lds[NW * 132 + 512 + 512];
  float* const l_mrg = lds;
  float* const l_hn = lds + NW * 132;
  float* const l_part = l_hn + 512;
  const int bid = blockIdx.x;
  const int h = (bid & 7) + 8 * ((bid >> 3) & 3), b = (bid >> 5) & 3, s = bid >> 7, g = 2 * h + s;
  const int tid = threadIdx.x, lane = tid & 63, w = __builtin_amdgcn_readfirstlane(tid >> 6), r = lane >> 4, c = lane & 15;
  if (w == 0) STAMP(0);
  const __amdgpu_buffer_rsrc_t rws = __builtin_amdgcn_make_buffer_rsrc((void*)ws, 0, WS_END, 0x00020000);

  const u32x4 xr = ld16(x + b * D_ + lane * 8);
  const u16 xs = x[b * D_ + 64 * w + 4 * c + r];
  SB();
  u32x4 WT[16];
  {
    const u16* wp = wq + (size_t)(64 * w + r) * (H_ * DH) + h * DH + 8 * c;
#pragma unroll
    for (int t = 0; t < 16; ++t) { WT[t] = ld16(wp + (size_t)4 * t * (H_ * DH)); SB(); }
  }
  const size_t base = ((size_t)(b * H_ + h) * SK + (size_t)s * (SK / 2)) * DH;
  const __amdgpu_buffer_rsrc_t rk = __builtin_amdgcn_make_buffer_rsrc((void*)(kc + base), 0, CHUNK_B, 0x00020000);
  const __amdgpu_buffer_rsrc_t rv = __builtin_amdgcn_make_buffer_rsrc((void*)(vc + base), 0, CHUNK_B, 0x00020000);
  const int voff = lane * 16;
  int so = w * GK * 1024;
#define TOFF(i) ((((i) / GK) * NW * GK + ((i) % GK)) * 1024)
#define LDN(j, sb) __builtin_amdgcn_raw_buffer_load_b128(((j) < T) ? rk : rv, voff + (TOFF((j) % T) & 4095), (sb) + (TOFF((j) % T) & ~4095), AUX_NT)
  u32x4 a[W];
#pragma unroll
  for (int j = 0; j < W; ++j) { a[j] = LDN(j, so); SB(); }
  const int sl = g < NSL ? g : 0;
  u32x4 WG[8], WD[4];
  {
    const u16* wp = ((c & 4) ? wu : wg) + (size_t)(64 * w + 4 * (c >> 3) + r) * F_ + 32 * sl + 8 * (c & 3);
#pragma unroll
    for (int t = 0; t < 8; ++t) { WG[t] = ld16(wp + (size_t)8 * t * F_); SB(); }
    const u16* dp = wd + (size_t)(32 * sl + 4 * (c >> 3) + r) * D_ + 64 * w + 8 * (c & 7);
#pragma unroll
    for (int t = 0; t < 4; ++t) { WD[t] = ld16(dp + 8 * t * D_); SB(); }
  }
  const unsigned tag = __builtin_amdgcn_raw_buffer_load_b32(rws, 0, WS_EPOCH + b * 256, AUX_SC1) + 1u;
  const u16 xres = x[b * D_ + 8 * g + (c >> 1)];
  SB();

  u32x4 mask, mask2;
  {
    const unsigned hm = (c & 1) ? 0xffff0000u : 0x0000ffffu;
#pragma unroll
    for (int i = 0; i < 4; ++i) { mask2[i] = (((c & 7) >> 1) == i) ? hm : 0u; mask[i] = (c < 8) ? mask2[i] : 0u; }
  }
  u32x4 q;
  {
    float v[8];
#pragma unroll
    for (int j = 0; j < 4; ++j) { v[2 * j] = bflo(xr[j]); v[2 * j + 1] = bfhi(xr[j]); }
    float mean, den;
    ln_stats(v, mean, den);
    const float xn = rb(bf2f(xs) - mean) / den;
    const unsigned pp = cvtpk(xn, xn);
    f32x4 acc = {0.f, 0.f, 0.f, 0.f};
    const u16* wop = wo + (size_t)(h * DH + 4 * (c >> 3) + r) * D_ + 64 * w + 8 * (c & 7);
    static_for<0, 16>([&](auto I) {
      constexpr int t = I.v;
      acc = mfma(band(dppu<BCAST + t>(pp), mask), WT[t], acc);
      SB();
      WT[t] = ld16(wop + 8 * t * D_);
      SB();
    });
    if (lane < 32) *(f32x4*)(l_mrg + w * 128 + 8 * c + 4 * r) = acc;
    __syncthreads();
    f32x4 a0 = {0.f, 0.f, 0.f, 0.f}, a1 = {0.f, 0.f, 0.f, 0.f};
#pragma unroll
    for (int ww = 0; ww < NW; ++ww) { a0 += *(const f32x4*)(l_mrg + ww * 128 + 8 * c); a1 += *(const f32x4*)(l_mrg + ww * 128 + 8 * c + 4); }
    q = u32x4{cvtpk(a0[0], a0[1]), cvtpk(a0[2], a0[3]), cvtpk(a1[0], a1[1]), cvtpk(a1[2], a1[3])};
  }
  if (w == 0) STAMP(1);

  float m = -INFINITY, l = 0.f;
  f32x4 acc = {0.f, 0.f, 0.f, 0.f};
  auto tile = [&](auto LAST) {
    constexpr bool last = LAST.v != 0;
    const int sn = so + STEP_B;
    float xx[T];
    static_for<0, T>([&](auto I) {
      constexpr int j = I.v;
      float d = 0.f;
#pragma unroll
      for (int k = 0; k < 4; ++k) d = dot2(a[j % W][k], q[k], d);
      xx[j] = d;
      asm volatile("" : "+v"(xx[j]));
      SB();
      if constexpr (j + W < 2 * T) a[j % W] = LDN(j + W, so);
      else if constexpr (!last) a[j % W] = LDN(j + W - 2 * T, sn);
      SB();
    });
#define TR_STEP(HALF, CTRL, BIT) _Pragma("unroll") for (int hh = 0; hh < (HALF); ++hh) { \
      const float t1_ = xx[hh] + dppf<CTRL>(xx[hh]), t2_ = xx[hh + (HALF)] + dppf<CTRL>(xx[hh + (HALF)]); \
      xx[hh] = (c & (BIT)) ? t2_ : t1_; }
    if constexpr (T == 16) { TR_STEP(8, XOR8, 8) TR_STEP(4, XOR7, 4) TR_STEP(2, XOR2, 2) TR_STEP(1, XOR1, 1) }
    else { TR_STEP(4, XOR8, 8) TR_STEP(2, XOR7, 4) TR_STEP(1, XOR2, 2) xx[0] += dppf<XOR1>(xx[0]); }
#undef TR_STEP
    const float sc = rb(rb(xx[0]) * 0.08838834764831845f);
    float tm = sc;
    if constexpr (T == 16) tm = fmaxf(tm, dppf<XOR1>(tm));
    tm = fmaxf(tm, dppf<XOR2>(tm)); tm = fmaxf(tm, dppf<XOR7>(tm)); tm = fmaxf(tm, dppf<XOR8>(tm));
    const float mn = fmaxf(m, xrow_max(tm));
    const float alpha = __builtin_amdgcn_exp2f((m - mn) * LOG2E);
    const float p = __builtin_amdgcn_exp2f((sc - mn) * LOG2E);
    m = mn;
    l = l * alpha + p;
    acc *= alpha;
    const unsigned pp = cvtpk(p, p);
    SB();
    static_for<T, 2 * T>([&](auto I) {
      constexpr int j = I.v;
      acc = mfma(band(dppu<BCAST + (j - T) * (16 / T)>(pp), mask), a[j % W], acc);
      SB();
      if constexpr (j + W < 2 * T) a[j % W] = LDN(j + W, so);
      else if constexpr (!last) a[j % W] = LDN(j + W - 2 * T, sn);
      SB();
    });
    so = sn;
  };
  for (int it = 0; it < NTILE - 1; ++it) tile(IC<0>{});
  tile(IC<1>{});
  if (w == 0) STAMP(2);

  l = wave_sum(l) * (T == 16 ? 1.0f : 0.5f);
  __syncthreads();
  if (lane < 32) *(f32x4*)(l_mrg + w * 132 + 4 + 8 * c + 4 * r) = acc;
  if (lane == 0) *(f32x2*)(l_mrg + w * 132) = f32x2{m, l};
  __syncthreads();
  if (w == 0) STAMP(3);
  {
    f32x2 ml[NW];
    float M = -INFINITY;
#pragma unroll
    for (int ww = 0; ww < NW; ++ww) { ml[ww] = *(const f32x2*)(l_mrg + ww * 132); M = fmaxf(M, ml[ww][0]); }
    float L = 0.f, o0 = 0.f, o1 = 0.f;
#pragma unroll
    for (int ww = 0; ww < NW; ++ww) {
      const float f = __builtin_amdgcn_exp2f((ml[ww][0] - M) * LOG2E);
      L += f * ml[ww][1];
      o0 += f * l_mrg[ww * 132 + 4 + 4 * c + r];
      o1 += f * l_mrg[ww * 132 + 4 + 64 + 4 * c + r];
    }
    const float inv = 1.0f / L;
    o0 *= inv; o1 *= inv;
    const unsigned p0 = cvtpk(o0, o0), p1 = cvtpk(o1, o1);
    f32x4 y = {0.f, 0.f, 0.f, 0.f};
    static_for<0, 16>([&](auto I) {
      constexpr int t = I.v;
      y = mfma(band(bcast2<(2 * t) & 15>(t < 8 ? p0 : p1), mask2), WT[t], y);
    });
    y = fold_bd(y, lane);
    const bool isy = c < 8;
    u32x4 pc;
    pc[0] = __float_as_uint(isy ? (r < 2 ? y[0] : y[2]) : M);
    pc[2] = __float_as_uint(isy ? (r < 2 ? y[1] : y[3]) : L);
    pc[1] = tag; pc[3] = tag;
    const int piece = isy ? 2 * (r & 1) + (r >> 1) : 4;
    if (isy || r == 0)
      __builtin_amdgcn_raw_buffer_store_b128(pc, rws, ((8 * w + (c & 7)) * 5 + piece) * 1024 + g * 16, WS_YB + b * (64 * 5 * 1024), AUX_SC1);
  }
  if (w == 0) STAMP(4);

  float hres = 0.f;
  if (w == 7) {
    u32x4 v[5];
    sweep<5>(rws, voff, WS_YB + (b * 64 + g) * (5 * 1024), tag, true, v);
    STAMP(5);
    const float m0 = __uint_as_float(v[4][0]), l0 = __uint_as_float(v[4][2]);
    const float m1 = dppf<XOR1>(m0);
    const float e0 = __builtin_amdgcn_exp2f((m0 - fmaxf(m0, m1)) * LOG2E) * l0;
    const float wt = e0 / (e0 + dppf<XOR1>(e0));
    float z[8];
#pragma unroll
    for (int i = 0; i < 4; ++i) { z[2 * i] = wt * __uint_as_float(v[i][0]); z[2 * i + 1] = wt * __uint_as_float(v[i][2]); }
    const float at = reduce8(z, c);
    hres = rb(bf2f(xres) + rb(at));
    const unsigned pair = cvtpk(hres, dppf<XOR2>(hres));
    if ((c & 2) == 0)
      __builtin_amdgcn_raw_buffer_store_b64(u32x2{pair, tag}, rws, (2 * r + (c & 1)) * 2048 + (4 * g + (c >> 2)) * 8, WS_HB + b * (REP * 2048), AUX_SC1);
    STAMP(6);
  }
  if (g < NSL) {
    if (w == 0) {
      u32x4 v[2];
      sweep<2>(rws, voff, WS_HB + (b * REP + (g & (REP - 1))) * 2048, tag, true, v);
      STAMP(7);
      float hv[8];
#pragma unroll
      for (int i = 0; i < 2; ++i) { hv[4 * i] = bflo(v[i][0]); hv[4 * i + 1] = bfhi(v[i][0]); hv[4 * i + 2] = bflo(v[i][2]); hv[4 * i + 3] = bfhi(v[i][2]); }
      float mean, den;
      ln_stats(hv, mean, den);
#pragma unroll
      for (int i = 0; i < 2; ++i)
        *(f32x4*)(l_hn + 256 * i + 4 * lane) = f32x4{rb(hv[4 * i] - mean) / den, rb(hv[4 * i + 1] - mean) / den, rb(hv[4 * i + 2] - mean) / den, rb(hv[4 * i + 3] - mean) / den};
    }
    __syncthreads();
    {
      const float hn = l_hn[64 * w + 4 * c + r];
      const unsigned pp = cvtpk(hn, hn);
      f32x4 gu = {0.f, 0.f, 0.f, 0.f};
      static_for<0, 8>([&](auto I) { constexpr int t = I.v; gu = mfma(band(bcast2<2 * t>(pp), mask2), WG[t], gu); });
      gu = fold_bd(gu, lane);
      if (lane < 32 && c < 8) *(f32x4*)(l_part + w * 64 + 8 * c + 4 * r) = gu;
    }
    __syncthreads();
    {
      float val = 0.f;
#pragma unroll
      for (int ww = 0; ww < NW; ++ww) val += l_part[ww * 64 + 4 * c + r];
      val = rb(val);
      const float oth = dppf<XOR8>(val);
      const float gg = c < 8 ? val : oth, uu = c < 8 ? oth : val;
      const float sg = rb(1.0f / (1.0f + __builtin_amdgcn_exp2f(-gg * LOG2E)));
      const float hid = sg * uu;
      const unsigned pp = cvtpk(hid, hid);
      f32x4 y = {0.f, 0.f, 0.f, 0.f};
      static_for<0, 4>([&](auto I) { constexpr int t = I.v; y = mfma(band(bcast2<2 * t>(pp), mask2), WD[t], y); });
      y = fold_bd(y, lane);
      u32x4 pc;
      pc[0] = __float_as_uint(r < 2 ? y[0] : y[2]);
      pc[2] = __float_as_uint(r < 2 ? y[1] : y[3]);
      pc[1] = tag; pc[3] = tag;
      if (c < 8)
        __builtin_amdgcn_raw_buffer_store_b128(pc, rws, ((8 * w + c) * 4 + 2 * (r & 1) + (r >> 1)) * 1024 + g * 16, WS_PB + b * (64 * 4 * 1024), AUX_SC1);
    }
    if (w == 0) STAMP(8);
  }
  if (w == 7) {
    u32x4 v[4];
    const bool act = lane < NSL;
    sweep<4>(rws, voff, WS_PB + (b * 64 + g) * (4 * 1024), tag, act, v);
    STAMP(9);
    float z[8];
#pragma unroll
    for (int i = 0; i < 4; ++i) { z[2 * i] = act ? __uint_as_float(v[i][0]) : 0.f; z[2 * i + 1] = act ? __uint_as_float(v[i][2]) : 0.f; }
    const float o = hres + rb(reduce8(z, c));
    const unsigned pair = cvtpk(o, dppf<XOR2>(o));
    if (lane < 16 && (c & 3) == 0) *(unsigned*)(out + b * D_ + 8 * g + (c >> 1)) = pair;
    if (g == 0 && lane == 0) __builtin_amdgcn_raw_buffer_store_b32(tag, rws, 0, WS_EPOCH + b * 256, AUX_SC1);
    STAMP(10);
  }
}

extern "C" void kernel_launch(void* const* d_in, const int* in_sizes, int n_in, void* d_out, int out_size, void* d_ws,
                              size_t ws_size, hipStream_t stream) {
  fused_kernel<<<256, 512, 0, stream>>>((const u16*)d_in[0], (const u16*)d_in[1], (const u16*)d_in[2], (const u16*)d_in[3],
                                        (const u16*)d_in[4], (const u16*)d_in[6], (const u16*)d_in[7], (const u16*)d_in[8],
                                        (u16*)d_out, (unsigned char*)d_ws);
}
```

```cpp
#include <hip/hip_runtime.h>
#include <hip/hip_bf16.h>
#include <stdint.h>

typedef unsigned u32x4 __attribute__((ext_vector_type(4)));
typedef unsigned u32x8 __attribute__((ext_vector_type(8)));
typedef __bf16 bf16x16 __attribute__((ext_vector_type(16)));
typedef unsigned u32x2 __attribute__((ext_vector_type(2)));
typedef float f32x4 __attribute__((ext_vector_type(4)));
typedef float f32x2 __attribute__((ext_vector_type(2)));
typedef __bf16 bf16x8 __attribute__((ext_vector_type(8)));
typedef __bf16 bf16x2 __attribute__((ext_vector_type(2)));
typedef unsigned long long u64x8 __attribute__((ext_vector_type(8)));
typedef unsigned long long u64x2 __attribute__((ext_vector_type(2)));
typedef unsigned short u16;

constexpr int D_ = 512, H_ = 32, SK = 8192, DH = 128, F_ = 1792;
constexpr int NW = 8, T = 16;
constexpr int HEAD_B = SK * DH * 2;
constexpr int NLD = 2 * (HEAD_B / 2) / (NW * 1024);
constexpr int NTILE = NLD / 2 / T;
constexpr int NSL = F_ / 32;
constexpr int REP = 8;
constexpr float LOG2E = 1.4426950408889634f;

constexpr int W = 4;
constexpr int PW = 36, IL = 28;
constexpr int WA = 12, WAT = 64;
constexpr int WE = 12, WEAT = 192;
constexpr int T0 = 315, TAU = 7300;
constexpr int WS0 = 35, SP = 6, LAG = 16;
constexpr int MLAT = 192;

constexpr int sched0(int n) {
  if (n - PW < WA) return n - PW;
  if (n < WAT) return n - WA;
  const int ramp = WEAT + (n - WEAT - W) / 2;
  return n < WEAT + W ? n - W : n - WE > ramp ? n - WE : ramp;
}
struct Sched {
  int first[NLD + 2];
  constexpr Sched() : first() {
    int n = PW, at = 0;
    for (int G = 0; G <= NLD + 1; ++G) {
      while (n < NLD && (at = sched0(n) > at ? sched0(n) : at) < G) ++n;
      first[G] = n;
    }
  }
};
constexpr Sched SCHED;
constexpr int first_at(int G) { return SCHED.first[G]; }
constexpr int dueof(int n) { return T0 + n * (TAU - T0) / NLD; }
constexpr int weight_at(int G) { return G >= WS0 && (G - WS0) % SP == 0 && (G - WS0) / SP < 28 ? (G - WS0) / SP : -2; }

constexpr int WS_EPOCH = 0;
constexpr int WS_ORG = 1024;
constexpr int WS_HB = 1024 + 8 * 4096;
constexpr int WS_ML = WS_HB + 4 * REP * 1024;
constexpr int WS_FL = WS_ML + 4 * 32 * 256;
constexpr int WS_YB = WS_FL + 4 * 256;
constexpr int WS_END = WS_YB + 4 * 64 * 4096;
constexpr unsigned EMPTY = 0xffffffffu;
constexpr int AUX_SC1 = 16, AUX_NT = 2;

constexpr int OROW = 288;
constexpr int L_Q = 0;
constexpr int L_O = L_Q + NW * 512;
constexpr int L_HN = L_O + NW * OROW;
constexpr int L_PART = L_HN + 1280;
constexpr int L_CS = L_PART + NW * 256;
constexpr int L_S = L_CS + NW * 256;
constexpr int L_END = L_S + 16;

template <int N> struct IC { static constexpr int v = N; };
template <int I, int N, typename F> __device__ __forceinline__ void static_for(F&& f) {
  if constexpr (N - I == 1) f(IC<I>{});
  else if constexpr (I < N) { static_for<I, (I + N) / 2>(f); static_for<(I + N) / 2, N>(f); }
}

__device__ __forceinline__ u16* gptr(unsigned long long a) { return (u16*)(__attribute__((address_space(1))) u16*)a; }
template <typename V> __device__ __forceinline__ V sel(bool c, V a, V b) { return c ? a : b; }
__device__ __forceinline__ float bf2f(u16 u) { return __uint_as_float((unsigned)u << 16); }
__device__ __forceinline__ float bflo(unsigned u) { return __uint_as_float(u << 16); }
__device__ __forceinline__ float bfhi(unsigned u) { return __uint_as_float(u & 0xffff0000u); }
__device__ __forceinline__ unsigned cvtpk(float lo, float hi) {
  return __builtin_bit_cast(unsigned, __builtin_convertvector(f32x2{lo, hi}, bf16x2));
}
__device__ __forceinline__ float rb(float x) { return __uint_as_float(cvtpk(0.f, x)); }
__device__ __forceinline__ float dot2(unsigned k, unsigned q, float acc) {
  return __builtin_amdgcn_fdot2_f32_bf16(__builtin_bit_cast(bf16x2, k), __builtin_bit_cast(bf16x2, q), acc, false);
}
template <int CTRL> __device__ __forceinline__ float dppf(float x) {
  return __builtin_bit_cast(float, __builtin_amdgcn_mov_dpp(__builtin_bit_cast(int, x), CTRL, 0xf, 0xf, true));
}
template <int CTRL> __device__ __forceinline__ unsigned dppu(unsigned x) {
  return (unsigned)__builtin_amdgcn_mov_dpp((int)x, CTRL, 0xf, 0xf, true);
}
constexpr int XOR1 = 0xB1, XOR2 = 0x4E, XOR7 = 0x141, XOR8 = 0x128, ROR4 = 0x124, BCAST = 0x150;
__device__ __forceinline__ float xrow_max(float x) {
  auto s = __builtin_amdgcn_permlane16_swap(__float_as_uint(x), __float_as_uint(x), false, false);
  x = fmaxf(__uint_as_float(s[0]), __uint_as_float(s[1]));
  auto t = __builtin_amdgcn_permlane32_swap(__float_as_uint(x), __float_as_uint(x), false, false);
  return fmaxf(__uint_as_float(t[0]), __uint_as_float(t[1]));
}
__device__ __forceinline__ float xrow_sum(float x) {
  auto s = __builtin_amdgcn_permlane16_swap(__float_as_uint(x), __float_as_uint(x), false, false);
  x = __uint_as_float(s[0]) + __uint_as_float(s[1]);
  auto t = __builtin_amdgcn_permlane32_swap(__float_as_uint(x), __float_as_uint(x), false, false);
  return __uint_as_float(t[0]) + __uint_as_float(t[1]);
}
__device__ __forceinline__ float row_sum(float x) {
  x += dppf<XOR1>(x); x += dppf<XOR2>(x); x += dppf<XOR7>(x); x += dppf<XOR8>(x);
  return x;
}
__device__ __forceinline__ float row_max(float x) {
  x = fmaxf(x, dppf<XOR1>(x)); x = fmaxf(x, dppf<XOR2>(x)); x = fmaxf(x, dppf<XOR7>(x)); x = fmaxf(x, dppf<XOR8>(x));
  return x;
}
__device__ __forceinline__ float wave_sum(float x) { return xrow_sum(row_sum(x)); }
__device__ __forceinline__ unsigned filled(unsigned v) { return min(v, EMPTY - 1u); }
__device__ __forceinline__ f32x4 mfma(u32x4 a, u32x4 b, f32x4 c) {
  return __builtin_amdgcn_mfma_f32_16x16x32_bf16(__builtin_bit_cast(bf16x8, a), __builtin_bit_cast(bf16x8, b), c, 0, 0, 0);
}
__device__ __forceinline__ f32x4 smfma(u32x4 a, u32x8 b, f32x4 c, int idx) {
  return __builtin_amdgcn_smfmac_f32_16x16x64_bf16(__builtin_bit_cast(bf16x8, a), __builtin_bit_cast(bf16x16, b), c, idx, 0, 0);
}
__device__ __forceinline__ u32x4 ld16(const void* p) { return *(const u32x4*)p; }
__device__ __forceinline__ u32x4 band(unsigned b, const u32x4& m) { return u32x4{b & m[0], b & m[1], b & m[2], b & m[3]}; }
#define SB() __builtin_amdgcn_sched_barrier(0)
__device__ __forceinline__ f32x2 pair_sum(f32x2 a, f32x2 b) {
  auto s = __builtin_amdgcn_permlane32_swap(__float_as_uint(a[0]), __float_as_uint(b[0]), false, false);
  auto t = __builtin_amdgcn_permlane32_swap(__float_as_uint(a[1]), __float_as_uint(b[1]), false, false);
  float x = __uint_as_float(s[0]) + __uint_as_float(s[1]), y = __uint_as_float(t[0]) + __uint_as_float(t[1]); SB();
  x += dppf<XOR2>(x); y += dppf<XOR2>(y); SB();
  x += dppf<ROR4>(x); y += dppf<ROR4>(y); SB();
  x += dppf<XOR8>(x); y += dppf<XOR8>(y); SB();
  s = __builtin_amdgcn_permlane16_swap(__float_as_uint(x), __float_as_uint(x), false, false);
  t = __builtin_amdgcn_permlane16_swap(__float_as_uint(y), __float_as_uint(y), false, false);
  return f32x2{__uint_as_float(s[0]) + __uint_as_float(s[1]), __uint_as_float(t[0]) + __uint_as_float(t[1])};
}

__device__ __forceinline__ void ln_stats(const float (&v)[8], float& mean_r, float& rden) {
  const float sh = __builtin_bit_cast(float, __builtin_amdgcn_readfirstlane(__builtin_bit_cast(int, v[0])));
  float s = 0.f, q = 0.f;
#pragma unroll
  for (int j = 0; j < 8; ++j) { const float d = v[j] - sh; s += d; q += d * d; }
  const float md = wave_sum(s) * (1.0f / 512.0f);
  const float var = fmaxf(wave_sum(q) * (1.0f / 512.0f) - md * md, 0.f);
  mean_r = rb(sh + md);
  rden = __builtin_amdgcn_rcpf(rb(__builtin_amdgcn_sqrtf(rb(rb(var) + 1e-5f))));
}
__device__ __forceinline__ int fresh0() { int z = 0; asm volatile("" : "+s"(z) :: "memory"); return z; }
__device__ __forceinline__ void sweep(const __amdgpu_buffer_rsrc_t& rs, int voff, int soff, unsigned tag, u32x4& v) {
  for (int spin = 0; spin < (1 << 22); ++spin) {
    v = __builtin_amdgcn_raw_buffer_load_b128(rs, voff, fresh0() + soff, AUX_SC1);
    if (__builtin_amdgcn_ballot_w64((v[1] != tag) | (v[3] != tag)) == 0) break;
  }
}
typedef unsigned u32x16 __attribute__((ext_vector_type(16)));
#define RN(k, i, j, n) "v[176+8*" #k "+4*" #i "+" #j ":176+8*" #k "+4*" #i "+" #j "+" #n "-1]"
#define R1(k, i, j) "v[176+8*" #k "+4*" #i "+" #j "]"
#define LD(k, i) "buffer_load_dwordx4 " RN(k, i, 0, 4) ", %[vo], %[rs], %[so] offen offset:1024*" #i " sc1\n"
#define NEXT "v_cmp_eq_u32 vcc, -1, %[a]\n s_and_b64 exec, exec, vcc\n s_cbranch_scc0 2f\n"
#define PASS2(k) LD(k, 0) LD(k, 1)
#define STEP2(k) "s_waitcnt vmcnt(2)\n" \
  "v_max3_u32 %[a], " R1(k, 0, 0) ", " R1(k, 0, 1) ", " R1(k, 0, 2) "\n v_max3_u32 %[b], " R1(k, 0, 3) ", " R1(k, 1, 0) ", " R1(k, 1, 1) "\n" \
  "v_max3_u32 %[a], %[a], " R1(k, 1, 2) ", " R1(k, 1, 3) "\n v_pk_add_f32 %[z0], " RN(k, 0, 0, 2) ", " RN(k, 1, 0, 2) "\n" \
  "v_max_u32 %[a], %[a], %[b]\n v_pk_add_f32 %[z1], " RN(k, 0, 2, 2) ", " RN(k, 1, 2, 2) "\n" NEXT PASS2(k)
#define STEP1(k) "s_waitcnt vmcnt(1)\n" \
  "v_max3_u32 %[a], " R1(k, 0, 0) ", " R1(k, 0, 1) ", " R1(k, 0, 2) "\n v_mov_b64 %[z0], " RN(k, 0, 0, 2) "\n" \
  "v_max_u32 %[a], %[a], " R1(k, 0, 3) "\n v_mov_b64 %[z1], " RN(k, 0, 2, 2) "\n" NEXT LD(k, 0)
#define GAP "s_sleep 7\n"
#define HEAD "s_mov_b64 %[sv], exec\n s_mov_b32 %[n], 0x200000\n"
#define TAIL "s_sub_u32 %[n], %[n], 1\n s_cbranch_scc0 1b\n 2:\n s_mov_b64 exec, %[sv]\n s_nop 1\n"
#define POOL "+{v[176:191]}"(pool)
#define SWEEP(body, soff_, z0_, z1_) do { unsigned a_, b_, n_; unsigned long long sv_; \
  asm volatile(body : [z0] "=&v"(z0_), [z1] "=&v"(z1_), [a] "=&v"(a_), [b] "=&v"(b_), [n] "=&s"(n_), [sv] "=&s"(sv_), POOL \
               : [vo] "v"(voff), [rs] "s"(rws), [so] "s"(soff_) : "vcc", "scc", "memory"); } while (0)
#define SUM2 HEAD PASS2(0) GAP PASS2(1) "1:\n" STEP2(0) STEP2(1) TAIL
#define GET1 HEAD LD(0, 0) GAP LD(1, 0) "1:\n" STEP1(0) STEP1(1) TAIL

__global__ void __launch_bounds__(512, 2)
fused_kernel(const u16*, const u16*, const u16*, const u16*, const u16*, const u16*, const u16*, const u16*, u16*, unsigned char*) {
  __shared__ __attribute__((aligned(16))) unsigned char lds[L_END];
  u64x8 ka; u64x2 kz;
  unsigned long long tst;
  asm volatile("s_load_dwordx16 %0, %3, 0\n s_load_dwordx4 %1, %3, 64\n s_memrealtime %2"
               : "=&s"(ka), "=&s"(kz), "=&s"(tst) : "s"(__builtin_amdgcn_kernarg_segment_ptr()));
  SB();
  const int bid = blockIdx.x;
  const int xj = (bid >> 3) & 3, xd = bid & 7;
  const int h = xd + 8 * xj;
  const int b = (bid >> 5) & 3, s = bid >> 7, g = 8 * xd + 2 * xj + s;
  const bool mlp = h < NSL / 2;
  const int tid = threadIdx.x, lane = tid & 63, w = __builtin_amdgcn_readfirstlane(tid >> 6), r = lane >> 4, c = lane & 15;
  unsigned cpf = 0;
  {
    unsigned long long pc; int len;
    asm volatile("s_getpc_b64 %0\n.Lfk_begin:\n s_mov_b32 %1, .Lfk_end-.Lfk_begin" : "=s"(pc), "=s"(len));
    const __amdgpu_buffer_rsrc_t rc = __builtin_amdgcn_make_buffer_rsrc((void*)pc, 0, len, 0x00020000);
    cpf = __builtin_amdgcn_raw_buffer_load_b32(rc, lane * 32768, (w * 32 + (bid >> 3)) * 128, 0);
    SB();
  }
  const unsigned phase = ((bid >> 3) & 15) * 7;
  const int voff = lane * 16, lead = ((bid >> 3) == 0) & (w == 1);
  const int so = (s * NW + w) * 4096;
  const int sl = 2 * (8 * xj + (xd & 4) + ((xd + xj) & 3)) + s;
  unsigned po0, po1, xo = b * 1024 + 128 * w + 8 * c + 2 * r, xro = b * 1024 + voff;
  {
    const int j = (2 * b + s) * 4 + (w & 3), n = 2 * b + (w & 1);
    po0 = (lane >> 2) * sel(w < 4, 8192, 256) + (lane & 3) * 64 + sel(w < 4, j * (16 * 8192) + h * 256, h * 131072 + j * 4096);
    const int c1 = sel(!mlp, 0, sel(w < 4, n * (64 * F_ * 2) + 64 * sl, sl * 32768 + n * 4096));
    po1 = lane * sel((!mlp) | (w >= 4), 0, F_ * 2) + sel(w < 6, c1, sel(w == 6, b * 1024 + 16 * g, so));
  }
  int to = sel(w < 4, WS_YB + (b * 64 + g) * 4096 + w * 1024 + voff, sel((w == 4) & (lane < REP), WS_HB + (b * 64 + g) * (16 * REP) + voff, WS_END));
  unsigned ev = sel((w == 3) & ((lane & 12) == 12), 0u, EMPTY);
  int qv = r * (H_ * DH * 2) + 16 * c, qs = 64 * w * (H_ * DH * 2) + h * DH * 2;
  unsigned long long kb = (unsigned long long)(b * H_ + h) * HEAD_B;
  asm volatile("" :: "v"(voff), "v"(po0), "v"(po1), "v"(xo), "v"(xro), "v"(to), "v"(ev), "v"(qv), "s"(qs), "s"(so), "s"(kb), "v"(lead));
  SB();
  asm volatile("s_waitcnt lgkmcnt(0)" : "+s"(ka), "+s"(kz), "+s"(tst) : "s"(__builtin_amdgcn_kernarg_segment_ptr()));
  SB();
  const u16 *const x = gptr(ka[0]), *const kc = gptr(ka[1]), *const vc = gptr(ka[2]), *const wq = gptr(ka[3]), *const wo = gptr(ka[4]);
  const u16 *const wg = gptr(ka[5]), *const wu = gptr(ka[6]), *const wd = gptr(ka[7]);
  u16* const out = gptr(kz[0]);
  unsigned char* const ws = (unsigned char*)gptr(kz[1]);
  unsigned t0 = (unsigned)tst - phase;

  const __amdgpu_buffer_rsrc_t rk = __builtin_amdgcn_make_buffer_rsrc((char*)kc + kb, 0, HEAD_B, 0x00020000);
  const __amdgpu_buffer_rsrc_t rv = __builtin_amdgcn_make_buffer_rsrc((char*)vc + kb, 0, HEAD_B, 0x00020000);
  const __amdgpu_buffer_rsrc_t rws = __builtin_amdgcn_make_buffer_rsrc((void*)ws, 0, WS_END, 0x00020000);
  u32x4 kv[NLD];
  auto pace = [&](unsigned due) {
    while ((int)((unsigned)__builtin_amdgcn_s_memrealtime() - due) < 0) __builtin_amdgcn_s_sleep(1);
    SB();
  };
  auto ldp = [&](auto N) {
    constexpr int n = N.v, i = n % (NLD / 2);
    if constexpr (n % 4 == 0 && n >= PW) pace(t0 + dueof(n));
    kv[n] = __builtin_amdgcn_raw_buffer_load_b128(n < NLD / 2 ? rk : rv, voff + (i & 3) * 1024, so + (i >> 2) * 65536, AUX_NT);
    SB();
  };
  if (lead) {
    unsigned pv;
    asm volatile("s_load_dword %0, %1, 0\n s_waitcnt lgkmcnt(0)" : "=s"(pv) : "s"((const char*)kc + kb + so));
    unsigned long long ta = __builtin_amdgcn_s_memrealtime();
    ta += ta > tst + 85 ? (unsigned)(ta - tst - 85) * 3 / 8 : 0;
    if (lane < 8) __builtin_amdgcn_raw_buffer_store_b64(__builtin_bit_cast(u32x2, ta), rws, lane * 4096, WS_ORG + xd * 8, AUX_SC1);
  }
  SB();
  ldp(IC<0>{});
  const unsigned pf0 = *(const unsigned*)((const char*)sel(w < 4, wq, wo) + po0);
  SB();
  const u32x4 xr = ld16((const char*)x + xro);
  const u16 xs = *(const u16*)((const char*)x + xo);
  SB();
  const unsigned pf1 = *(const unsigned*)((const char*)sel(w < 4, sel(w < 2, wg, wu), sel(w < 6, wd, sel(w == 6, (const u16*)out, vc + kb / 2))) + po1);
  SB();
  __builtin_amdgcn_raw_buffer_store_b128(u32x4{ev, ev, ev, ev}, rws, to, 0, AUX_SC1);
  SB();
  u32x4 WF[16];
  const __amdgpu_buffer_rsrc_t rq = __builtin_amdgcn_make_buffer_rsrc((void*)wq, 0, D_ * H_ * DH * 2, 0x00020000);
  static_for<0, 16>([&](auto I) {
    constexpr int t = I.v;
    if constexpr (t % 4 == 0) { __builtin_amdgcn_s_barrier(); SB(); }
    WF[t] = __builtin_amdgcn_raw_buffer_load_b128(rq, qv, qs + 4 * t * (H_ * DH * 2), 0); SB();
    static_for<1 + t * (IL - 1) / 16, 1 + (t + 1) * (IL - 1) / 16>(ldp);
  });
  __builtin_amdgcn_s_barrier(); SB();
  const u32x2 org = __builtin_amdgcn_raw_buffer_load_b64(rws, w == 0 ? (lane & 7) * 8 : WS_END, WS_ORG + xd * 4096, AUX_SC1);
  SB();
  static_for<IL, PW>(ldp);
  const unsigned tag = __builtin_amdgcn_raw_buffer_load_b32(rws, 0, WS_EPOCH + b * 256, AUX_SC1) + 1u;
  const unsigned xres = *(const unsigned*)(x + b * D_ + 8 * g + 4 * (c & 1) + (r & 2));
  SB();
  u32x4 mask;
  u32x4 E1, E2;
  {
    const unsigned hm = (c & 1) ? 0xffff0000u : 0x0000ffffu;
#pragma unroll
    for (int i = 0; i < 4; ++i) {
      const unsigned m2 = (((c & 7) >> 1) == i) ? hm : 0u;
      mask[i] = (c < 8) ? m2 : 0u;
      E1[i] = (r == (c >> 3)) ? (m2 & 0x3f803f80u) : 0u;
      E2[i] = (r == 2 + (c >> 3)) ? (m2 & 0x3f803f80u) : 0u;
    }
  }

  u32x4 q;
  {
    float v[8];
#pragma unroll
    for (int j = 0; j < 4; ++j) { v[2 * j] = bflo(xr[j]); v[2 * j + 1] = bfhi(xr[j]); }
    float mean, rden;
    ln_stats(v, mean, rden);
    const float xn = rb(bf2f(xs) - mean) * rden;
    unsigned px = cvtpk(xn, xn);
    asm volatile("" : "+v"(px), "+v"(mask));
    SB();
    f32x4 a = {0.f, 0.f, 0.f, 0.f};
    static_for<0, 16>([&](auto I) { a = mfma(band(dppu<BCAST + I.v>(px), mask), WF[I.v], a); SB(); });
    if (lane < 32) *(f32x4*)(lds + L_Q + w * 512 + 32 * c + 16 * r) = a;
    if (w == 0) {
      const unsigned long long d = tst + 400u - __builtin_bit_cast(unsigned long long, org);
      const unsigned e0 = d < 800u ? (unsigned)d : 0u;
      unsigned a = e0 + (e0 ? 1u << 16 : 0u);
      a += dppu<XOR1>(a); a += dppu<XOR2>(a); a += dppu<XOR7>(a);
      const unsigned sh = a ? 300u - (unsigned)((float)(a & 0xffffu) * __builtin_amdgcn_rcpf((float)(a >> 16))) + ((unsigned)tst & 7) : 0u;
      *(unsigned*)(lds + L_S) = sh;
    }
    __syncthreads();
    f32x4 a0 = {0.f, 0.f, 0.f, 0.f}, a1 = {0.f, 0.f, 0.f, 0.f};
#pragma unroll
    for (int ww = 0; ww < NW; ++ww) { a0 += *(const f32x4*)(lds + L_Q + ww * 512 + 32 * c); a1 += *(const f32x4*)(lds + L_Q + ww * 512 + 32 * c + 16); }
    t0 += __builtin_amdgcn_readfirstlane(*(const unsigned*)(lds + L_S));
    q = u32x4{cvtpk(a0[0], a0[1]), cvtpk(a0[2], a0[3]), cvtpk(a1[0], a1[1]), cvtpk(a1[2], a1[3])};
  }
  asm volatile("" :: "v"(pf0), "v"(pf1), "v"(cpf));

  const int wrow = 16 * (c >> 3) + 4 * ((c >> 2) & 1) + (r >> 1), wcol = 64 * w + 32 * (r & 1) + 8 * (c & 3);
  const u16* const wop = wo + (h * DH + wrow) * D_ + wcol;
  const u16* const wgp = (r & 1 ? wu : wg) + (mlp ? (64 * w + wrow) * F_ + 32 * sl + 8 * (c & 3) : 0);
  const int wdo = (32 * sl + 8 * (c >> 2) + (r >> 1)) * D_ + wcol;
  const u16* const wdp = wd + (((mlp) & (w == 7)) ? wdo : 0);
  u32x8 WT[12];
  u32x4 WD[4];
  auto transpose = [&](auto P, auto O) {
    constexpr int p = P.v;
    const f32x4 z = {0.f, 0.f, 0.f, 0.f};
#pragma unroll
    for (int f = 0; f < 4; ++f) {
      const f32x4 a1 = mfma(WF[O.v + f], E1, z), a2 = mfma(WF[O.v + f], E2, z);
#pragma unroll
      for (int i = 0; i < 4; ++i) {
        if constexpr (p < 6) WT[4 * (p >> 1) + i][4 * (p & 1) + f] = cvtpk(a1[i], a2[i]);
        else WD[i][f] = cvtpk(a1[i], a2[i]);
      }
    }
    if constexpr (p < 6) asm volatile("" : "+v"(WT[4 * (p >> 1)]), "+v"(WT[4 * (p >> 1) + 1]), "+v"(WT[4 * (p >> 1) + 2]), "+v"(WT[4 * (p >> 1) + 3]));
    else asm volatile("" : "+v"(WD[0]), "+v"(WD[1]), "+v"(WD[2]), "+v"(WD[3]));
  };
  auto wload = [&](auto I) {
    constexpr int i = I.v, fo = 8 * (i & 1) + (i & 2);
    WF[i & 7] = ld16(i < 16 ? wop + (32 * (i >> 2) + fo) * D_ : i < 24 ? wgp + (32 * ((i >> 2) & 1) + fo) * F_ : wdp + 2 * (i & 3) * D_);
    SB();
  };
  const int sidx = c & 1 ? (int)0xeeeeeeeeu : 0x44444444;
  f32x4 y[4];
  auto clear = [&]() {
#pragma unroll
    for (int i = 0; i < 4; ++i) y[i] = f32x4{0.f, 0.f, 0.f, 0.f};
    asm volatile("" : "+v"(y[0]), "+v"(y[1]), "+v"(y[2]), "+v"(y[3]));
  };
  auto gemv_gu = [&](const u32x4& a) {
#pragma unroll
    for (int i = 0; i < 4; ++i) y[i] = smfma(a, WT[8 + i], y[i], sidx);
    SB();
  };
  auto put_gu = [&](int dst, float off) {
    float e0 = y[0][0] + y[0][1], e1 = y[1][0] + y[1][1], e2 = y[2][0] + y[2][1], e3 = y[3][0] + y[3][1];
    asm volatile("" : "+v"(e0), "+v"(e1), "+v"(e2), "+v"(e3) : "v"(y[0]), "v"(y[1]), "v"(y[2]), "v"(y[3]));
    *(float*)(lds + dst) = (r < 2 ? (r ? e1 : e0) : (r == 2 ? e2 : e3)) - off;
  };
  auto sum8 = [&](int src) {
    float v[8];
#pragma unroll
    for (int ww = 0; ww < NW; ++ww) v[ww] = *(const float*)(lds + src + 4 * lane + ww * 256);
    const f32x2 t = (f32x2{v[0], v[1]} + f32x2{v[2], v[3]}) + (f32x2{v[4], v[5]} + f32x2{v[6], v[7]});
    return t[0] + t[1];
  };

  static_assert(WS0 + 27 * SP + LAG < NLD && LAG < 5 * SP, "");
  auto refill = [&](auto I) {
    constexpr int G = I.v, i = weight_at(G), p = weight_at(G - LAG);
    SB();
    static_for<first_at(G), first_at(G + 1)>(ldp);
    if constexpr (i >= 0) wload(IC<i>{});
    if constexpr (p >= 0 && p % 4 == 3) { transpose(IC<p / 4>{}, IC<(p & 4)>{}); SB(); }
    if constexpr (G == NLD - 6) { clear(); SB(); }
    if constexpr (G == NLD - 2) {
      pace(t0 + phase + dueof(NLD - 4) + 45);
      const u16* const dp = wd + (((mlp) & (w != 7)) ? wdo : 0);
      static_for<0, 4>([&](auto J) { WF[8 + J.v] = ld16(dp + 2 * J.v * D_); SB(); });
    }
    if constexpr (p == 25) {
      const u32x4 one = {0x3f803f80u, 0x3f803f80u, 0x3f803f80u, 0x3f803f80u};
      clear(); gemv_gu(one); put_gu(L_CS + w * 256 + 4 * lane, 0.f);
      SB();
    }
  };
  float sc[NTILE];
  static_for<0, NTILE>([&](auto TI) {
    float xx[T];
    static_for<0, T>([&](auto I) {
      constexpr int j = I.v;
      const u32x4 kk = kv[TI.v * T + j];
      float d = 0.f;
#pragma unroll
      for (int k = 0; k < 4; ++k) d = dot2(kk[k], q[k], d);
      xx[j] = d;
      asm volatile("" : "+v"(xx[j]));
      refill(IC<TI.v * T + j>{});
    });
#define TR_STEP(HALF, CTRL, BIT) _Pragma("unroll") for (int hh = 0; hh < (HALF); ++hh) { \
      const float t1_ = xx[hh] + dppf<CTRL>(xx[hh]), t2_ = xx[hh + (HALF)] + dppf<CTRL>(xx[hh + (HALF)]); \
      xx[hh] = (c & (BIT)) ? t2_ : t1_; }
    TR_STEP(8, XOR8, 8) TR_STEP(4, XOR7, 4) TR_STEP(2, XOR2, 2) TR_STEP(1, XOR1, 1)
#undef TR_STEP
    sc[TI.v] = rb(rb(xx[0]) * 0.08838834764831845f);
    asm volatile("" : "+v"(sc[TI.v]));
    SB();
  });
  float m = sc[0];
#pragma unroll
  for (int t = 1; t < NTILE; ++t) m = fmaxf(m, sc[t]);
  m = xrow_max(row_max(m));
  unsigned pp[NTILE];
  {
    float l = 0.f;
#pragma unroll
    for (int t = 0; t < NTILE; ++t) { const float p = __builtin_amdgcn_exp2f((sc[t] - m) * LOG2E); l += p; pp[t] = cvtpk(p, p); }
    l = wave_sum(l);
    __builtin_amdgcn_raw_buffer_store_b128(u32x4{__float_as_uint(m), tag, __float_as_uint(l), tag}, rws, lane ? WS_END : 0, WS_ML + (b * H_ + h) * 256 + (s * NW + w) * 16, AUX_SC1);
    __builtin_amdgcn_raw_buffer_store_b32(tag, rws, tid ? WS_END : 0, WS_FL + b * 256 + g * 4, AUX_SC1);
  }
  SB();

  f32x4 acc = {0.f, 0.f, 0.f, 0.f};
  float fw = 0.f;
  u32x4 ml;
  unsigned ar;
  int ow = lane < 32 ? L_O + w * OROW + 16 * c + 8 * r : L_O + w * OROW + 256, ord = L_O + (c >> 1) * OROW + 32 * r + 16 * (c & 1);
  int ypos = (8 * w + 4 * (c >> 3) + r) * 4096 + g * 32 + (c & 7) * 4;
  int hrd = L_HN + 144 * (2 * r + (c & 1)) + 16 * w, pw = L_PART + w * 256 + 4 * lane, sw = L_S + 8 - 8 * (r & 1);
  asm volatile("" : "+v"(ow), "+v"(ord), "+v"(ypos), "+v"(hrd), "+v"(pw), "+v"(sw));
  if (w == 7) __builtin_amdgcn_s_setprio(0);
  static_for<NLD / 2, NLD>([&](auto I) {
    constexpr int G = I.v, j = G % T;
    acc = mfma(band(dppu<BCAST + j>(pp[G / T - NTILE]), mask), kv[G], acc);
    if constexpr (G == MLAT) {
      SB();
      ml = __builtin_amdgcn_raw_buffer_load_b128(rws, c * 16, WS_ML + (b * H_ + h) * 256, AUX_SC1);
      ar = __builtin_amdgcn_raw_buffer_load_b32(rws, w == 7 ? lane * 4 : WS_END, WS_FL + b * 256, AUX_SC1);
    }
    if constexpr (G == MLAT + W + 2) {
      SB();
      if (__builtin_amdgcn_ballot_w64((ml[1] != tag) | (ml[3] != tag))) sweep(rws, c * 16, WS_ML + (b * H_ + h) * 256, tag, ml);
      if (__builtin_amdgcn_ballot_w64((ar != tag) & (w == 7)))
        for (int spin = 0; spin < (1 << 22); ++spin)
          if (__builtin_amdgcn_ballot_w64(__builtin_amdgcn_raw_buffer_load_b32(rws, lane * 4, fresh0() + WS_FL + b * 256, AUX_SC1) != tag) == 0) break;
      const float mo = __uint_as_float(ml[0]), M = row_max(mo);
      const float L = row_sum(__builtin_amdgcn_exp2f((mo - M) * LOG2E) * __uint_as_float(ml[2]));
      fw = __builtin_amdgcn_exp2f((m - M) * LOG2E) * __builtin_amdgcn_rcpf(L);
      asm volatile("" : "+v"(fw));
    }
    refill(I);
  });

  *(u32x2*)(lds + ow) = u32x2{cvtpk(acc[0] * fw, acc[1] * fw), cvtpk(acc[2] * fw, acc[3] * fw)};
  __syncthreads();
  {
    const u32x4 a0 = *(const u32x4*)(lds + ord), a1 = *(const u32x4*)(lds + ord + 128);
    SB();
#pragma unroll
    for (int u = 0; u < 8; ++u) { y[u & 3] = smfma(u < 4 ? a0 : a1, WT[u], y[u & 3], sidx); SB(); }
    unsigned z[4];
#pragma unroll
    for (int i = 0; i < 4; ++i) z[i] = __float_as_uint((y[i][0] + y[i][2]) + (y[i][1] + y[i][3]));
    const auto s0 = __builtin_amdgcn_permlane16_swap(z[0], z[1], false, false), s1 = __builtin_amdgcn_permlane16_swap(z[2], z[3], false, false);
    const auto s2 = __builtin_amdgcn_permlane32_swap(__float_as_uint(__uint_as_float(s0[0]) + __uint_as_float(s0[1])),
                                                     __float_as_uint(__uint_as_float(s1[0]) + __uint_as_float(s1[1])), false, false);
    __builtin_amdgcn_raw_buffer_store_b32(filled(__float_as_uint(__uint_as_float(s2[0]) + __uint_as_float(s2[1]))), rws, ypos, WS_YB + b * (64 * 4096), AUX_SC1);
  }
  SB();
  clear();
  float cs = sum8(L_CS);
  asm volatile("" : "+v"(cs));
  SB();

  const int yb = WS_YB + (b * 64 + g) * 4096;
  float h0 = 0.f, h1 = 0.f;
  u32x16 pool;
  asm volatile("" : "={v[176:191]}"(pool));
  if (w != 7 && mlp) { transpose(IC<6>{}, IC<8>{}); SB(); }
  if (w == 7) {
    f32x2 z0, z1;
    const int hr = (c >> 1) + 8 * (r & 1);
    int hpos = hr >= REP ? WS_END : hr * 1024 + 128 * (g & 7) + 16 * xd + 8 * (c & 1) + 2 * (r & 2);
    float x0 = bflo(xres), x1 = bfhi(xres);
    asm volatile("" : "+v"(hpos), "+v"(x0), "+v"(x1));
    __builtin_amdgcn_s_setprio(3);
    __builtin_amdgcn_s_sleep(8);
    SWEEP(SUM2, yb, z0, z1);
    z0 = pair_sum(z0, z1);
    const unsigned hp = cvtpk(x0 + rb(z0[0]), x1 + rb(z0[1]));
    __builtin_amdgcn_raw_buffer_store_b32(filled(hp), rws, hpos, WS_HB + b * (REP * 1024), AUX_SC1);
    SB();
    __builtin_amdgcn_raw_buffer_store_b32(tag, rws, (g | lane) ? WS_END : 0, WS_EPOCH + b * 256, AUX_SC1);
    SB();
    h0 = bflo(hp); h1 = bfhi(hp);
  }
  if (mlp) {
    u32x4 v;
    if (w == 7) {
      u32x2 v0, v1;
      int hw = L_HN + voff + 16 * (lane >> 3);
      asm volatile("" : "+v"(hw));
      __builtin_amdgcn_s_sleep(12);
      SWEEP(GET1, WS_HB + (b * REP + xd) * 1024, v0, v1);
      v = u32x4{v0[0], v0[1], v1[0], v1[1]};
      *(u32x4*)(lds + hw) = v;
    }
    __syncthreads();
    {
      const u32x4 a0 = *(const u32x4*)(lds + hrd);
      float off = 0.f;
      if (w == 7) {
        float sm = dot2(v[0], 0x3f803f80u, 0.f), sq = dot2(v[0], v[0], 0.f), sn = dot2(v[1], 0x3f803f80u, 0.f), sr = dot2(v[1], v[1], 0.f);
        sm = dot2(v[2], 0x3f803f80u, sm); sq = dot2(v[2], v[2], sq); sn = dot2(v[3], 0x3f803f80u, sn); sr = dot2(v[3], v[3], sr);
        SB();
        sm += sn; sq += sr; SB();
        sm += dppf<XOR1>(sm); sq += dppf<XOR1>(sq); SB();
        sm += dppf<XOR2>(sm); sq += dppf<XOR2>(sq); SB();
        sm += dppf<XOR7>(sm); sq += dppf<XOR7>(sq); SB();
        sm += dppf<XOR8>(sm); sq += dppf<XOR8>(sq); SB();
        auto t = __builtin_amdgcn_permlane16_swap(__float_as_uint(sm), __float_as_uint(sq), false, false);
        float z = __uint_as_float(t[0]) + __uint_as_float(t[1]);
        SB();
        y[0] = smfma(a0, WT[8], y[0], sidx); SB();
        t = __builtin_amdgcn_permlane32_swap(__float_as_uint(z), __float_as_uint(z), false, false);
        z = (__uint_as_float(t[0]) + __uint_as_float(t[1])) * (1.0f / 512.0f); SB();
        y[1] = smfma(a0, WT[9], y[1], sidx); SB();
        const float mu = __builtin_bit_cast(float, __builtin_amdgcn_readfirstlane(__builtin_bit_cast(int, z))); SB();
        y[2] = smfma(a0, WT[10], y[2], sidx); SB();
        z = fmaxf(z - mu * mu, 0.f);
        off = rb(mu) * cs; SB();
        y[3] = smfma(a0, WT[11], y[3], sidx); SB();
        z = __builtin_amdgcn_sqrtf(rb(rb(z) + 1e-5f));
        asm volatile("" : "+v"(z)); SB();
        put_gu(pw, off); SB();
        *(float*)(lds + sw) = __builtin_amdgcn_rcpf(rb(z));
      } else {
        gemv_gu(a0);
        put_gu(pw, 0.f);
      }
    }
    __syncthreads();
    {
      const float rd = *(const float*)(lds + L_S);
      float val = sum8(L_PART);
      val = rb(val * rd);
      const float oth = dppf<XOR8>(val);
      const float hid = rb(__builtin_amdgcn_rcpf(1.0f + __builtin_amdgcn_exp2f(-val * LOG2E))) * oth;
      const unsigned pr = cvtpk(hid, dppf<XOR1>(hid));
      const u32x4 af = {dppu<BCAST>(pr), dppu<BCAST + 2>(pr), dppu<BCAST + 4>(pr), dppu<BCAST + 6>(pr)};
      const f32x4 z = {0.f, 0.f, 0.f, 0.f};
      const float d0 = mfma(af, WD[0], z)[0], d1 = mfma(af, WD[1], z)[0], d2 = mfma(af, WD[2], z)[0], d3 = mfma(af, WD[3], z)[0];
      const float yd = r < 2 ? (r ? d1 : d0) : (r == 2 ? d2 : d3);
      __builtin_amdgcn_raw_buffer_store_b32(filled(__float_as_uint(yd)), rws, ypos + 2048, WS_YB + b * (64 * 4096), AUX_SC1);
    }
  }
  if (w == 7) {
    f32x2 z0, z1;
    const __amdgpu_buffer_rsrc_t ro = __builtin_amdgcn_make_buffer_rsrc((char*)out + b * 1024 + 16 * g, 0, 16, 0x00020000);
    int opos = r & 1 ? 16 : 8 * c + 2 * r;
    asm volatile("" : "+v"(opos) : "s"(ro));
    __builtin_amdgcn_s_sleep(11);
    if (!mlp) __builtin_amdgcn_s_sleep(49);
    SWEEP(SUM2, yb + 2048, z0, z1);
    z0 = pair_sum(z0, z1);
    __builtin_amdgcn_raw_buffer_store_b32(cvtpk(h0 + rb(z0[0]), h1 + rb(z0[1])), ro, opos, 0, 0);
    asm volatile("s_waitcnt vmcnt(1)" : POOL :: "memory");
    asm volatile(".Lfk_end:");
  }
}

extern "C" void kernel_launch(void* const* d_in, const int* in_sizes, int n_in, void* d_out, int out_size, void* d_ws,
                              size_t ws_size, hipStream_t stream) {
  fused_kernel<<<256, 512, 0, stream>>>((const u16*)d_in[0], (const u16*)d_in[1], (const u16*)d_in[2], (const u16*)d_in[3],
                                        (const u16*)d_in[4], (const u16*)d_in[6], (const u16*)d_in[7], (const u16*)d_in[8],
                                        (u16*)d_out, (unsigned char*)d_ws);
}
```
